```python
import torch

def _softmax(x, axis=-1):
    return torch.softmax(x, dim=axis)
def _gelu(x):
    return torch.nn.functional.gelu(x, approximate="tanh")
def _silu(x):
    return torch.nn.functional.silu(x)
def _sigmoid(x):
    return torch.sigmoid(x)
def _layernorm(x, axis=-1, eps=1e-5):
    m = x.mean(dim=axis, keepdim=True)
    v = x.var(dim=axis, keepdim=True, unbiased=False)
    return (x - m) / torch.sqrt(v + eps)
def _scatter_add(tgt, idx, src, axis=0):
    return tgt.clone().index_add_(axis, idx, src)
def _segment_sum(src, seg, n):
    out = torch.zeros((n,) + src.shape[1:], dtype=src.dtype)
    return out.index_add_(0, seg, src)
def _pad(x, pw):
    flat = tuple(v for pair in reversed(pw) for v in pair)
    return torch.nn.functional.pad(x, flat)

kernel_name = 'sequential_preLN__gqa_block_cached__mixer_token_mlp__decode'

def setup_inputs():

    _w = (lambda a: (lambda t: t.to(torch.bfloat16) if t.is_floating_point() else t)(torch.as_tensor(a)))
    def _A_setup():
        B, Sq, D = 8, 1, 512
        Hq, Hkv, Sk, Dh = 32, 8, 8192, 128
        assert Hq % Hkv == 0, (Hq, Hkv)
        return {
            "x": _w((lambda *s: torch.randn(*s, dtype=torch.bfloat16) * 0.05)(B, Sq, D)),
            "k_cache": _w((lambda *s: torch.randn(*s, dtype=torch.bfloat16) * 1.6)(B, Hkv, Sk, Dh)),
            "v_cache": _w((lambda *s: torch.randn(*s, dtype=torch.bfloat16) * 0.8)(B, Hkv, Sk, Dh)),
            "wq": _w((lambda *s: torch.randn(*s, dtype=torch.bfloat16) * 1.6)(D, Hq * Dh) * (D ** -0.5)),
            "wo": _w((lambda *s: torch.randn(*s, dtype=torch.bfloat16) * 0.8)(Hq * Dh, D) * ((Hq * Dh) ** -0.5)),
        }
    _A_in = _A_setup()

    def _B_setup():
        B, S, D, F = 8, 1, 512, 1792
        return {
            "x": _w((lambda *s: torch.randn(*s, dtype=torch.bfloat16) * 0.05)(B, S, D)),
            "w1": _w((lambda *s: torch.randn(*s, dtype=torch.bfloat16) * 1.6)(S, F) * (S ** -0.5)),
            "b1": _w((lambda *s: torch.randn(*s, dtype=torch.bfloat16) * 1.6)(F) * 0.02),
            "w2": _w((lambda *s: torch.randn(*s, dtype=torch.bfloat16) * 1.6)(F, S) * (F ** -0.5)),
            "b2": _w((lambda *s: torch.randn(*s, dtype=torch.bfloat16) * 0.8)(S) * 0.02),
        }
    _B_in = _B_setup()

    return {
        "A_x": _A_in["x"],
        "A_k_cache": _A_in["k_cache"],
        "A_v_cache": _A_in["v_cache"],
        "A_wq": _A_in["wq"],
        "A_wo": _A_in["wo"],
        "B_x": _B_in["x"],
        "B_w1": _B_in["w1"],
        "B_b1": _B_in["b1"],
        "B_w2": _B_in["w2"],
        "B_b2": _B_in["b2"],
    }

def reference(A_x, A_k_cache, A_v_cache, A_wq, A_wo, B_x, B_w1, B_b1, B_w2, B_b2):
    def _A_ref(x, k_cache, v_cache, wq, wo):
        B, Sq, D = x.shape
        Hq, Dh = 32, 128
        Hkv = k_cache.shape[1]
        rep = Hq // Hkv
        q = (lambda x, *d: x.permute(*d))((x @ wq).reshape(B, Sq, Hq, Dh), 0, 2, 1, 3)
        k = (lambda x, n, axis: torch.repeat_interleave(x, n, dim=axis))(k_cache, rep, axis=1)
        v = (lambda x, n, axis: torch.repeat_interleave(x, n, dim=axis))(v_cache, rep, axis=1)
        scores = torch.einsum("bhqd,bhkd->bhqk", q, k) * (Dh ** -0.5)
        p = _softmax(scores, axis=-1)
        o = torch.einsum("bhqk,bhkd->bhqd", p, v)
        return (lambda x, *d: x.permute(*d))(o, 0, 2, 1, 3).reshape(B, Sq, Hq * Dh) @ wo

    def _B_ref(x, w1, b1, w2, b2):
        B, S, D = x.shape
        h = _layernorm(x, axis=-1)
        u = torch.einsum("bsd,sf->bdf", h, w1) + b1
        u = _gelu(u)
        y = torch.einsum("bdf,fs->bds", u, w2) + b2
        return (lambda x, *d: x.permute(*d))(y, 0, 2, 1)

    x = A_x
    h = x + _A_ref(_layernorm(x, axis=-1), A_k_cache, A_v_cache, A_wq, A_wo)
    h2 = h + _B_ref(_layernorm(h, axis=-1), B_w1, B_b1, B_w2, B_b2)
    return h2
```

```cpp
#include <hip/hip_runtime.h>
#include <hip/hip_bf16.h>
#include <stdint.h>

#define LAS __attribute__((address_space(3)))
#define GAS __attribute__((address_space(1)))
typedef unsigned v4u __attribute__((ext_vector_type(4)));
typedef unsigned v2u __attribute__((ext_vector_type(2)));
typedef int v4i __attribute__((ext_vector_type(4)));
typedef float f32x4 __attribute__((ext_vector_type(4)));
typedef short s16x4 __attribute__((ext_vector_type(4)));
typedef __bf16 bf16x8 __attribute__((ext_vector_type(8)));
typedef __bf16 bf16x2 __attribute__((ext_vector_type(2)));
typedef unsigned long long u64;

#define NT_RING 4
#define STAMPS 0

namespace {

constexpr int NB = 8, DM = 512, HQ = 32, HKV = 8, SK = 8192, DH = 128, FF = 1792;
constexpr int NWG = 256, NTHR = 512;
constexpr int NT = NT_RING;
constexpr int TILES_PER_WAVE = 32;
static_assert(TILES_PER_WAVE % NT == 0, "ring must divide the tile count");
constexpr float LN_EPS = 1e-5f;
constexpr float SCALE_LOG2E = 0.08838834764831845f * 1.4426950408889634f;

constexpr unsigned WS_EPOCH = 0;
constexpr unsigned WS_FLAG = 4096;
constexpr unsigned WS_H = 8192;
constexpr unsigned WS_Q = 32768;
constexpr unsigned REC_BYTES = 2304;
constexpr unsigned WS_REC = WS_Q + NB * 4096 * 8;
constexpr unsigned WS_STAMP = WS_REC + NWG * REC_BYTES;
constexpr unsigned WS_END = WS_STAMP + NWG * 16 * 8;

constexpr unsigned L_RING = 0;
constexpr unsigned L_STAGE = 4 * NT * 8192;
constexpr unsigned L_QBUF = L_STAGE + 16384;
constexpr unsigned L_PART = L_QBUF + 1024;
constexpr unsigned L_ML = L_PART + 8192;
constexpr unsigned L_STAT = L_ML + 128;
constexpr unsigned L_Y = L_STAT + 64;
constexpr unsigned L_T = L_Y + 256;
constexpr unsigned L_MLP = L_T + 128;
constexpr unsigned L_CTL = L_MLP + 256;
constexpr unsigned L_END = L_CTL + 128;
static_assert(L_END <= 160 * 1024, "LDS budget");
enum { C_QRDY = 0, C_STAT = 1, C_MERGE = 2, C_YCNT = 3, C_STGFREE = 4, C_TLO = 6, C_THI = 7, C_TRDY = 8 };

constexpr unsigned SPIN_MAX = 1u << 18;

struct Params {
  const unsigned short* x; const unsigned short* k; const unsigned short* v; const unsigned short* wq; const unsigned short* wo;
  const unsigned short* w1; const unsigned short* b1; const unsigned short* w2; const unsigned short* b2;
  unsigned short* out; unsigned char* ws;
};

__device__ __forceinline__ v4i mkrsrc(const void* p, unsigned bytes) {
  const u64 a = (u64)p;
  v4i r; r.x = __builtin_amdgcn_readfirstlane((int)(unsigned)a); r.y = __builtin_amdgcn_readfirstlane((int)((unsigned)(a >> 32) & 0xffffu));
  r.z = (int)bytes; r.w = 0x00020000; return r;
}
__device__ __forceinline__ void dma4_nt(v4i rs, unsigned v0, unsigned v1, unsigned v2, unsigned v3, unsigned soff, unsigned lds) {
  asm volatile("s_mov_b32 m0, %6\n\ts_nop 0\n\t"
               "buffer_load_dwordx4 %0, %4, %5 offen nt lds\n\t"
               "buffer_load_dwordx4 %1, %4, %5 offen offset:1024 nt lds\n\t"
               "buffer_load_dwordx4 %2, %4, %5 offen offset:2048 nt lds\n\t"
               "buffer_load_dwordx4 %3, %4, %5 offen offset:3072 nt lds"
               :: "v"(v0), "v"(v1), "v"(v2), "v"(v3), "s"(rs), "s"(soff), "s"(lds) : "memory");
}
__device__ __forceinline__ void dma1(v4i rs, unsigned voff, unsigned soff, unsigned lds) {
  asm volatile("s_mov_b32 m0, %3\n\ts_nop 0\n\t"
               "buffer_load_dwordx4 %0, %1, %2 offen lds"
               :: "v"(voff), "s"(rs), "s"(soff), "s"(lds) : "memory");
}
template <int N> __device__ __forceinline__ void wait_vm() { asm volatile("s_waitcnt vmcnt(%0)" :: "i"(N) : "memory"); }
__device__ __forceinline__ void wait_lgkm() { asm volatile("s_waitcnt lgkmcnt(0)" ::: "memory"); }
__device__ __forceinline__ void wg_barrier() { asm volatile("s_waitcnt lgkmcnt(0)\n\ts_barrier" ::: "memory"); }

template <int CTRL> __device__ __forceinline__ float dpp(float x) {
  return __builtin_bit_cast(float, __builtin_amdgcn_mov_dpp(__builtin_bit_cast(int, x), CTRL, 0xf, 0xf, true));
}
constexpr int DPP_XOR1 = 0xB1, DPP_XOR2 = 0x4E, DPP_HMIRROR = 0x141, DPP_ROR8 = 0x128, DPP_ROR4 = 0x124;
__device__ __forceinline__ float xrow_max(float x) {
  auto s = __builtin_amdgcn_permlane16_swap(__float_as_uint(x), __float_as_uint(x), false, false);
  x = fmaxf(__uint_as_float(s[0]), __uint_as_float(s[1]));
  auto t = __builtin_amdgcn_permlane32_swap(__float_as_uint(x), __float_as_uint(x), false, false);
  return fmaxf(__uint_as_float(t[0]), __uint_as_float(t[1]));
}
__device__ __forceinline__ float xrow_sum(float x) {
  auto s = __builtin_amdgcn_permlane16_swap(__float_as_uint(x), __float_as_uint(x), false, false);
  x = __uint_as_float(s[0]) + __uint_as_float(s[1]);
  auto t = __builtin_amdgcn_permlane32_swap(__float_as_uint(x), __float_as_uint(x), false, false);
  return __uint_as_float(t[0]) + __uint_as_float(t[1]);
}
__device__ __forceinline__ float row16_sum(float x) {
  x += dpp<DPP_XOR1>(x); x += dpp<DPP_XOR2>(x); x += dpp<DPP_HMIRROR>(x); x += dpp<DPP_ROR8>(x); return x;
}
__device__ __forceinline__ float wave_sum(float x) { return xrow_sum(row16_sum(x)); }
__device__ __forceinline__ unsigned pk_bf16(float lo, float hi) {
  bf16x2 r; r[0] = (__bf16)lo; r[1] = (__bf16)hi; return __builtin_bit_cast(unsigned, r);
}
__device__ __forceinline__ float bf_lo(unsigned u) { return __uint_as_float(u << 16); }
__device__ __forceinline__ float bf_hi(unsigned u) { return __uint_as_float(u & 0xffff0000u); }
__device__ __forceinline__ float bf1(unsigned short u) { return __uint_as_float((unsigned)u << 16); }
__device__ __forceinline__ f32x4 mfma32(v4u a, v4u b, f32x4 c) {
  return __builtin_amdgcn_mfma_f32_16x16x32_bf16(__builtin_bit_cast(bf16x8, a), __builtin_bit_cast(bf16x8, b), c, 0, 0, 0);
}
__device__ __forceinline__ f32x4 mfma16(v2u a, v2u b, f32x4 c) {
  return __builtin_amdgcn_mfma_f32_16x16x16bf16_1k(__builtin_bit_cast(s16x4, a), __builtin_bit_cast(s16x4, b), c, 0, 0, 0);
}
__device__ __forceinline__ v2u tr_read(LAS unsigned char* p) {
  return __builtin_bit_cast(v2u, __builtin_amdgcn_ds_read_tr16_b64_v4i16((LAS s16x4*)p));
}
__device__ __forceinline__ v4u ld_sc1(__amdgpu_buffer_rsrc_t rs, unsigned off) { return __builtin_amdgcn_raw_buffer_load_b128(rs, (int)off, 0, 16); }
__device__ __forceinline__ void st_sc1(__amdgpu_buffer_rsrc_t rs, unsigned off, v4u v) { __builtin_amdgcn_raw_buffer_store_b128(v, rs, (int)off, 0, 16); }
__device__ __forceinline__ unsigned lds_ld(LAS unsigned char* L, int idx) { return *(volatile LAS unsigned*)(L + L_CTL + 4 * idx); }
__device__ __forceinline__ void lds_st(LAS unsigned char* L, int idx, unsigned v) { *(volatile LAS unsigned*)(L + L_CTL + 4 * idx) = v; }
__device__ __forceinline__ void lds_wait(LAS unsigned char* L, int idx, unsigned want) {
  for (unsigned s = 0; s < SPIN_MAX; ++s) { if (lds_ld(L, idx) >= want) break; __builtin_amdgcn_s_sleep(1); }
  asm volatile("" ::: "memory");
}
__device__ __forceinline__ unsigned tagf(float f) { return __float_as_uint(f) | 1u; }
#define STAMP(i) do { } while (0)

struct AttnState { float m, l; f32x4 o[8]; };

template <int WK, int WV, bool ISSUE>
__device__ __forceinline__ void attn_tile(AttnState& st, LAS unsigned char* L, unsigned slot, const v4u (&qf)[4],
                                          const unsigned (&ka)[4], const unsigned (&va)[8],
                                          v4i rk, v4i rv, const unsigned (&vk)[4], const unsigned (&vv)[2], unsigned next_off) {
  wait_vm<WK>();
  v4u kf[4];
#pragma unroll
  for (int s = 0; s < 4; ++s) kf[s] = *(LAS v4u*)(L + slot + ka[s]);
  wait_lgkm();
  if (ISSUE) dma4_nt(rk, vk[0], vk[1], vk[2], vk[3], next_off, slot);
  f32x4 sc = {0.f, 0.f, 0.f, 0.f};
#pragma unroll
  for (int s = 0; s < 4; ++s) sc = mfma32(kf[s], qf[s], sc);
  wait_vm<WV>();
  v2u vf[8];
#pragma unroll
  for (int c = 0; c < 8; ++c) vf[c] = tr_read(L + slot + 4096 + va[c]);
  float s0 = sc[0] * SCALE_LOG2E, s1 = sc[1] * SCALE_LOG2E, s2 = sc[2] * SCALE_LOG2E, s3 = sc[3] * SCALE_LOG2E;
  float tm = xrow_max(fmaxf(fmaxf(s0, s1), fmaxf(s2, s3)));
  if (__builtin_amdgcn_ballot_w64(tm > st.m) != 0ull) {
    const float mn = fmaxf(st.m, tm);
    const float al = __builtin_amdgcn_exp2f(st.m - mn);
    st.m = mn; st.l *= al;
    const float a0 = dpp<0x00>(al), a1 = dpp<0x55>(al), a2 = dpp<0xAA>(al), a3 = dpp<0xFF>(al);
#pragma unroll
    for (int c = 0; c < 8; ++c) { st.o[c][0] *= a0; st.o[c][1] *= a1; st.o[c][2] *= a2; st.o[c][3] *= a3; }
  }
  const float p0 = __builtin_amdgcn_exp2f(s0 - st.m), p1 = __builtin_amdgcn_exp2f(s1 - st.m);
  const float p2 = __builtin_amdgcn_exp2f(s2 - st.m), p3 = __builtin_amdgcn_exp2f(s3 - st.m);
  st.l += (p0 + p1) + (p2 + p3);
  v2u pf; pf.x = pk_bf16(p0, p1); pf.y = pk_bf16(p2, p3);
  wait_lgkm();
  if (ISSUE) dma4_nt(rv, vv[0], vv[1], vv[0], vv[1], next_off, slot + 4096);
#pragma unroll
  for (int c = 0; c < 8; ++c) st.o[c] = mfma16(pf, vf[c], st.o[c]);
}

template <int R> struct TailTiles {
  static __device__ __forceinline__ void run(AttnState& st, LAS unsigned char* L, unsigned ring, const v4u (&qf)[4], const unsigned (&ka)[4], const unsigned (&va)[8],
                                             v4i rk, v4i rv, const unsigned (&vk)[4], const unsigned (&vv)[2]) {
    attn_tile<8 * (NT - 1 - R) + 4, 8 * (NT - 1 - R), false>(st, L, ring + R * 8192, qf, ka, va, rk, rv, vk, vv, 0u);
    if constexpr (R + 1 < NT) TailTiles<R + 1>::run(st, L, ring, qf, ka, va, rk, rv, vk, vv);
  }
};

__global__ void __launch_bounds__(NTHR, 2) fused_decode_kernel(Params p) {
  extern __shared__ __attribute__((aligned(16))) unsigned char lds_raw[];
  LAS unsigned char* L = (LAS unsigned char*)lds_raw;
  const int tid = threadIdx.x, lane = tid & 63, wave = __builtin_amdgcn_readfirstlane(tid >> 6);
  const int c = blockIdx.x;
  const int b = c >> 5, kvh = (c >> 2) & 7, sp = c & 3, grp = c >> 2;
  const int cq = (c & 7) * 32 + (c >> 3);
  const int cg = 4 * (c & 7) + ((c >> 3) & 3);
  const int g = lane >> 4, c16 = lane & 15;
  const __amdgpu_buffer_rsrc_t rws = __builtin_amdgcn_make_buffer_rsrc(p.ws, 0, WS_END, 0x00020000);

  unsigned short w1r[7], b1r[7], w2r[7], b2r;
  auto load_params = [&]() {
    const int fg = tid & 255;
#pragma unroll
    for (int i = 0; i < 7; ++i) { w1r[i] = p.w1[fg + 256 * i]; b1r[i] = p.b1[fg + 256 * i]; w2r[i] = p.w2[fg + 256 * i]; }
    b2r = p.b2[0];
  };

  if (wave < 4) {
    if (wave == 0) STAMP(0);
    const v4i rk = mkrsrc(p.k, 1u << 27), rv = mkrsrc(p.v, 1u << 27);
    unsigned vk[4], vv[2], ka[4], va[8];
#pragma unroll
    for (int i = 0; i < 4; ++i) vk[i] = g * 256 + 16 * (c16 ^ (4 * i + g));
#pragma unroll
    for (int i = 0; i < 2; ++i) vv[i] = g * 256 + 16 * (c16 ^ (((4 * i + g) & 7) << 1));
#pragma unroll
    for (int s = 0; s < 4; ++s) ka[s] = 256 * c16 + 16 * ((4 * s + g) ^ c16);
    {
      const int q4 = c16 >> 2, p4 = lane & 3, row = 4 * g + q4;
#pragma unroll
      for (int cc = 0; cc < 8; ++cc) va[cc] = 256 * row + 16 * ((2 * cc + (p4 >> 1)) ^ ((row & 7) << 1)) + 8 * (p4 & 1);
    }
    const unsigned ring = L_RING + wave * (NT * 8192);
    const unsigned tbase = (unsigned)grp * (2048u * 1024u) + (unsigned)(sp * 4 + wave) * 4096u;
    wg_barrier();
#pragma unroll
    for (int r = 0; r < NT; ++r) {
      dma4_nt(rk, vk[0], vk[1], vk[2], vk[3], tbase + r * 65536u, ring + r * 8192);
      dma4_nt(rv, vv[0], vv[1], vv[0], vv[1], tbase + r * 65536u, ring + r * 8192 + 4096);
    }
    lds_wait(L, C_QRDY, 1u);
    if (wave == 0) STAMP(1);
    v4u qf[4];
#pragma unroll
    for (int s = 0; s < 4; ++s) qf[s] = *(LAS v4u*)(L + L_QBUF + (lane & 3) * 256 + s * 64 + g * 16);
    AttnState st; st.m = -INFINITY; st.l = 0.f;
#pragma unroll
    for (int cc = 0; cc < 8; ++cc) st.o[cc] = f32x4{0.f, 0.f, 0.f, 0.f};
    for (int j = 0; j < TILES_PER_WAVE - NT; j += NT) {
#pragma unroll
      for (int r = 0; r < NT; ++r)
        attn_tile<8 * NT - 4, 8 * NT - 4, true>(st, L, ring + r * 8192, qf, ka, va, rk, rv, vk, vv, tbase + (unsigned)(j + NT + r) * 65536u);
    }
    TailTiles<0>::run(st, L, ring, qf, ka, va, rk, rv, vk, vv);
    if (wave == 0) STAMP(2);
    load_params();
    const float lsum = xrow_sum(st.l);
    if (lane < 16) {
#pragma unroll
      for (int cc = 0; cc < 8; ++cc) *(LAS f32x4*)(L + L_PART + wave * 2048 + (16 * cc + lane) * 16) = st.o[cc];
      if (lane < 4) { *(LAS float*)(L + L_ML + wave * 32 + lane * 4) = st.m; *(LAS float*)(L + L_ML + wave * 32 + 16 + lane * 4) = lsum; }
    }
    wait_lgkm();
    unsigned old = 0;
    if (lane == 0) old = __hip_atomic_fetch_add((LAS unsigned*)(L + L_CTL + 4 * C_MERGE), 1u, __ATOMIC_RELAXED, __HIP_MEMORY_SCOPE_WORKGROUP);
    old = __builtin_amdgcn_readfirstlane(old);
    asm volatile("" ::: "memory");
    if (old == 3u) {
      f32x4 mw[4], lw[4];
#pragma unroll
      for (int w = 0; w < 4; ++w) { mw[w] = *(LAS f32x4*)(L + L_ML + w * 32); lw[w] = *(LAS f32x4*)(L + L_ML + w * 32 + 16); }
      f32x4 M, Ls = {0.f, 0.f, 0.f, 0.f}, a0 = {0.f, 0.f, 0.f, 0.f}, a1 = {0.f, 0.f, 0.f, 0.f};
#pragma unroll
      for (int h = 0; h < 4; ++h) M[h] = fmaxf(fmaxf(mw[0][h], mw[1][h]), fmaxf(mw[2][h], mw[3][h]));
#pragma unroll
      for (int w = 0; w < 4; ++w) {
        f32x4 f;
#pragma unroll
        for (int h = 0; h < 4; ++h) f[h] = __builtin_amdgcn_exp2f(mw[w][h] - M[h]);
        const f32x4 x0 = *(LAS f32x4*)(L + L_PART + w * 2048 + lane * 16), x1 = *(LAS f32x4*)(L + L_PART + w * 2048 + 1024 + lane * 16);
        Ls += f * lw[w]; a0 += f * x0; a1 += f * x1;
      }
      const unsigned rec = WS_REC + (unsigned)c * REC_BYTES;
      st_sc1(rws, rec + lane * 16, v4u{tagf(a0[0]), tagf(a0[1]), tagf(a0[2]), tagf(a0[3])});
      st_sc1(rws, rec + 1024 + lane * 16, v4u{tagf(a1[0]), tagf(a1[1]), tagf(a1[2]), tagf(a1[3])});
      if (lane < 2) { const f32x4 hv = lane ? Ls : M; st_sc1(rws, rec + 2048 + lane * 16, v4u{tagf(hv[0]), tagf(hv[1]), tagf(hv[2]), tagf(hv[3])}); }
      STAMP(3);
    }
  } else {
    const int hw = wave - 4;
    if (hw == 0) {
      const v4i rq = mkrsrc(p.wq, DM * HQ * DH * 2);
      const unsigned vo = (lane >> 1) * 8192 + (lane & 1) * 16;
#pragma unroll
      for (int s = 0; s < 16; ++s) dma1(rq, vo, (unsigned)cq * 32u + (unsigned)s * (32u * 8192u), L_STAGE + s * 1024);
      v4u xf[16];
      const GAS v4u* xp = (const GAS v4u*)(p.x + (c16 & 7) * DM + g * 8);
#pragma unroll
      for (int s = 0; s < 16; ++s) xf[s] = xp[s * 4];
      const u64 ep = __hip_atomic_load((GAS u64*)(p.ws + WS_EPOCH), __ATOMIC_RELAXED, __HIP_MEMORY_SCOPE_AGENT);
      const u64 T = ep + 0x9E3779B97F4A7C15ull;
      wg_barrier();
      if (lane == 0) { lds_st(L, C_TLO, (unsigned)T); lds_st(L, C_THI, (unsigned)(T >> 32)); wait_lgkm(); lds_st(L, C_TRDY, 1u); }
      if (c16 >= 8) {
#pragma unroll
        for (int s = 0; s < 16; ++s) xf[s] = v4u{0x3f803f80u, 0x3f803f80u, 0x3f803f80u, 0x3f803f80u};
      }
      wait_vm<0>();
      f32x4 d = {0.f, 0.f, 0.f, 0.f};
      const unsigned ta = (8 * g + (c16 >> 2)) * 32 + (lane & 3) * 8;
#pragma unroll
      for (int s = 0; s < 16; ++s) {
        const v2u lo = tr_read(L + L_STAGE + s * 1024 + ta), hi = tr_read(L + L_STAGE + s * 1024 + ta + 128);
        d = mfma32(v4u{lo.x, lo.y, hi.x, hi.y}, xf[s], d);
      }
      wait_lgkm();
      lds_st(L, C_STGFREE, 1u);
      lds_wait(L, C_STAT, 1u);
      const float mean = *(LAS float*)(L + L_STAT + (c16 & 7) * 8), rstd = *(LAS float*)(L + L_STAT + (c16 & 7) * 8 + 4);
      unsigned gl[4];
#pragma unroll
      for (int r = 0; r < 4; ++r) {
        const float cs = dpp<DPP_ROR8>(d[r]);
        const float q = rstd * (d[r] - mean * cs);
        gl[r] = (pk_bf16(q, q) & 0xffffu) | ((unsigned)T << 16);
      }
      const unsigned thi = (unsigned)(T >> 16);
      if (c16 < 8) {
        const unsigned off = WS_Q + ((unsigned)c16 * 4096u + (unsigned)cq * 16u + g * 4u) * 8u;
        st_sc1(rws, off, v4u{gl[0], thi, gl[1], thi});
        st_sc1(rws, off + 16, v4u{gl[2], thi, gl[3], thi});
      }
      STAMP(4);
      const unsigned qoff = WS_Q + ((unsigned)b * 4096u + (unsigned)kvh * 512u + lane * 8u) * 8u;
      const unsigned tlo = (unsigned)T << 16;
      v4u a0, a1, a2, a3;
      for (unsigned s = 0; s < SPIN_MAX; ++s) {
        asm volatile("" ::: "memory");
        a0 = ld_sc1(rws, qoff); a1 = ld_sc1(rws, qoff + 16); a2 = ld_sc1(rws, qoff + 32); a3 = ld_sc1(rws, qoff + 48);
        const unsigned bad = ((a0.x ^ tlo) >> 16) | ((a0.z ^ tlo) >> 16) | ((a1.x ^ tlo) >> 16) | ((a1.z ^ tlo) >> 16) |
                             ((a2.x ^ tlo) >> 16) | ((a2.z ^ tlo) >> 16) | ((a3.x ^ tlo) >> 16) | ((a3.z ^ tlo) >> 16) |
                             (a0.y ^ thi) | (a0.w ^ thi) | (a1.y ^ thi) | (a1.w ^ thi) | (a2.y ^ thi) | (a2.w ^ thi) | (a3.y ^ thi) | (a3.w ^ thi);
        if (__builtin_amdgcn_ballot_w64(bad != 0u) == 0ull) break;
      }
      *(LAS v4u*)(L + L_QBUF + lane * 16) = v4u{(a0.x & 0xffffu) | (a0.z << 16), (a1.x & 0xffffu) | (a1.z << 16), (a2.x & 0xffffu) | (a2.z << 16), (a3.x & 0xffffu) | (a3.z << 16)};
      wait_lgkm();
      lds_st(L, C_QRDY, 1u);
      STAMP(5);
    } else if (hw == 1) {
      v4u xv[8];
      const GAS v4u* xp = (const GAS v4u*)(p.x + (lane >> 3) * DM) + (lane & 7);
#pragma unroll
      for (int i = 0; i < 8; ++i) xv[i] = xp[i * 8];
      wg_barrier();
      float s = 0.f;
#pragma unroll
      for (int i = 0; i < 8; ++i)
#pragma unroll
        for (int e = 0; e < 4; ++e) s += bf_lo(xv[i][e]) + bf_hi(xv[i][e]);
      s += dpp<DPP_XOR1>(s); s += dpp<DPP_XOR2>(s); s += dpp<DPP_HMIRROR>(s);
      const float mean = s * (1.f / DM);
      float vs = 0.f;
#pragma unroll
      for (int i = 0; i < 8; ++i)
#pragma unroll
        for (int e = 0; e < 4; ++e) { const float d0 = bf_lo(xv[i][e]) - mean, d1 = bf_hi(xv[i][e]) - mean; vs = fmaf(d0, d0, vs); vs = fmaf(d1, d1, vs); }
      vs += dpp<DPP_XOR1>(vs); vs += dpp<DPP_XOR2>(vs); vs += dpp<DPP_HMIRROR>(vs);
      const float rstd = 1.f / sqrtf(vs * (1.f / DM) + LN_EPS);
      if ((lane & 7) == 0) { *(LAS float*)(L + L_STAT + (lane >> 3) * 8) = mean; *(LAS float*)(L + L_STAT + (lane >> 3) * 8 + 4) = rstd; }
      wait_lgkm();
      lds_st(L, C_STAT, 1u);
    } else if (hw == 2) {
      const v4u z = {0u, 0u, 0u, 0u};
      const unsigned rec = WS_REC + (unsigned)c * REC_BYTES;
      st_sc1(rws, rec + lane * 16, z); st_sc1(rws, rec + 1024 + lane * 16, z);
      if (lane < 2) st_sc1(rws, rec + 2048 + lane * 16, z);
      if (lane < 4) st_sc1(rws, WS_H + ((unsigned)b * 512u + (unsigned)cg * 16u) * 4u + lane * 16, z);
      wg_barrier();
      lds_wait(L, C_TRDY, 1u);
      const u64 T = (u64)lds_ld(L, C_TLO) | ((u64)lds_ld(L, C_THI) << 32);
      wait_vm<0>();
      if (lane == 0) __hip_atomic_store((GAS u64*)(p.ws + WS_FLAG) + c, T, __ATOMIC_RELAXED, __HIP_MEMORY_SCOPE_AGENT);
    } else {
      if (lane < 32) lds_st(L, lane, 0u);
      wg_barrier();
    }
    lds_wait(L, C_STGFREE, 1u);
    lds_wait(L, C_TRDY, 1u);
    const u64 T = (u64)lds_ld(L, C_TLO) | ((u64)lds_ld(L, C_THI) << 32);
    v4u wf[32];
    {
      const v4i ro = mkrsrc(p.wo, HQ * DH * DM * 2);
      const unsigned vo = (lane >> 1) * 1024 + (lane & 1) * 16;
      const unsigned stg = L_STAGE + hw * 4096;
      const unsigned ta = (8 * g + (c16 >> 2)) * 32 + (lane & 3) * 8;
#pragma unroll
      for (int rnd = 0; rnd < 8; ++rnd) {
#pragma unroll
        for (int s = 0; s < 4; ++s) dma1(ro, vo, (unsigned)cg * 32u + (unsigned)(hw * 32 + rnd * 4 + s) * (32u * 1024u), stg + s * 1024);
        wait_vm<0>();
#pragma unroll
        for (int s = 0; s < 4; ++s) {
          const v2u lo = tr_read(L + stg + s * 1024 + ta), hi = tr_read(L + stg + s * 1024 + ta + 128);
          wf[rnd * 4 + s] = v4u{lo.x, lo.y, hi.x, hi.y};
        }
        wait_lgkm();
      }
    }
    load_params();
    const v2u xr = *(const GAS v2u*)(p.x + b * DM + cg * 16 + (lane & 3) * 4);
    {
      const GAS u64* fp = (const GAS u64*)(p.ws + WS_FLAG) + b * 32 + (lane & 31);
      for (unsigned s = 0; s < SPIN_MAX; ++s) {
        const u64 f = __hip_atomic_load(fp, __ATOMIC_RELAXED, __HIP_MEMORY_SCOPE_AGENT);
        if (__builtin_amdgcn_ballot_w64(f != T) == 0ull) break;
        __builtin_amdgcn_s_sleep(8);
      }
      asm volatile("" ::: "memory");
    }
    if (hw == 0) STAMP(6);
    lds_wait(L, C_MERGE, 1u);
    f32x4 acc[4];
#pragma unroll
    for (int j = 0; j < 4; ++j) acc[j] = f32x4{0.f, 0.f, 0.f, 0.f};
    {
      const int sg = lane & 3, jq = (lane >> 2) & 3;
#pragma unroll
      for (int kk = 0; kk < 2; ++kk) {
        const unsigned rec = WS_REC + (unsigned)(b * 32 + (hw * 2 + kk) * 4 + sg) * REC_BYTES;
        const unsigned doff = rec + (32 * jq + 8 * g) * 16;
        v4u dv[8], hm, hl;
        for (unsigned s = 0; s < SPIN_MAX; ++s) {
          asm volatile("" ::: "memory");
          hm = ld_sc1(rws, rec + 2048); hl = ld_sc1(rws, rec + 2064);
#pragma unroll
          for (int d = 0; d < 8; ++d) dv[d] = ld_sc1(rws, doff + d * 16);
          unsigned ok = hm.x & hm.y & hm.z & hm.w & hl.x & hl.y & hl.z & hl.w;
#pragma unroll
          for (int d = 0; d < 8; ++d) ok &= dv[d].x & dv[d].y & dv[d].z & dv[d].w;
          if (__builtin_amdgcn_ballot_w64((ok & 1u) == 0u) == 0ull) break;
        }
#pragma unroll
        for (int h = 0; h < 4; ++h) {
          const float mh = __uint_as_float(hm[h]), lh = __uint_as_float(hl[h]);
          float mx = fmaxf(mh, dpp<DPP_XOR1>(mh)); mx = fmaxf(mx, dpp<DPP_XOR2>(mx));
          const float e = __builtin_amdgcn_exp2f(mh - mx);
          float lt = e * lh; lt += dpp<DPP_XOR1>(lt); lt += dpp<DPP_XOR2>(lt);
          const float w = e / lt;
          v4u af;
#pragma unroll
          for (int d = 0; d < 4; ++d) af[d] = pk_bf16(__uint_as_float(dv[2 * d][h]) * w, __uint_as_float(dv[2 * d + 1][h]) * w);
#pragma unroll
          for (int j = 0; j < 4; ++j) acc[j] = mfma32(af, wf[kk * 16 + h * 4 + j], acc[j]);
        }
      }
    }
    float ys;
    {
      const float s0 = (acc[0][0] + acc[0][1]) + (acc[0][2] + acc[0][3]), s1 = (acc[1][0] + acc[1][1]) + (acc[1][2] + acc[1][3]);
      const float s2 = (acc[2][0] + acc[2][1]) + (acc[2][2] + acc[2][3]), s3 = (acc[3][0] + acc[3][1]) + (acc[3][2] + acc[3][3]);
      ys = xrow_sum(g == 0 ? s0 : g == 1 ? s1 : g == 2 ? s2 : s3);
    }
    if (lane < 16) *(LAS float*)(L + L_Y + hw * 64 + lane * 4) = ys;
    wait_lgkm();
    unsigned old = 0;
    if (lane == 0) old = __hip_atomic_fetch_add((LAS unsigned*)(L + L_CTL + 4 * C_YCNT), 1u, __ATOMIC_RELAXED, __HIP_MEMORY_SCOPE_WORKGROUP);
    old = __builtin_amdgcn_readfirstlane(old);
    asm volatile("" ::: "memory");
    if (old == 3u) {
      STAMP(7);
      if (lane < 4) {
        const f32x4 y0 = *(LAS f32x4*)(L + L_Y + lane * 16), y1 = *(LAS f32x4*)(L + L_Y + 64 + lane * 16);
        const f32x4 y2 = *(LAS f32x4*)(L + L_Y + 128 + lane * 16), y3 = *(LAS f32x4*)(L + L_Y + 192 + lane * 16);
        const f32x4 y = (y0 + y1) + (y2 + y3);
        const f32x4 hv = {bf_lo(xr.x) + y[0], bf_hi(xr.x) + y[1], bf_lo(xr.y) + y[2], bf_hi(xr.y) + y[3]};
        st_sc1(rws, WS_H + ((unsigned)b * 512u + (unsigned)cg * 16u) * 4u + lane * 16, v4u{tagf(hv[0]), tagf(hv[1]), tagf(hv[2]), tagf(hv[3])});
      }
      if (c == 0 && lane == 0) __hip_atomic_store((GAS u64*)(p.ws + WS_EPOCH), T, __ATOMIC_RELAXED, __HIP_MEMORY_SCOPE_AGENT);
      const unsigned hoff = WS_H + (unsigned)b * 2048u + lane * 16;
      v4u h0, h1;
      for (unsigned s = 0; s < SPIN_MAX; ++s) {
        asm volatile("" ::: "memory");
        h0 = ld_sc1(rws, hoff); h1 = ld_sc1(rws, hoff + 1024);
        const unsigned ok = h0.x & h0.y & h0.z & h0.w & h1.x & h1.y & h1.z & h1.w;
        if (__builtin_amdgcn_ballot_w64((ok & 1u) == 0u) == 0ull) break;
      }
      float hv[8];
#pragma unroll
      for (int e = 0; e < 4; ++e) { hv[e] = __uint_as_float(h0[e]); hv[4 + e] = __uint_as_float(h1[e]); }
      float z[8];
      {
        float s = 0.f;
#pragma unroll
        for (int e = 0; e < 8; ++e) s += hv[e];
        const float m1 = wave_sum(s) * (1.f / DM);
        float vs = 0.f;
#pragma unroll
        for (int e = 0; e < 8; ++e) { z[e] = hv[e] - m1; vs = fmaf(z[e], z[e], vs); }
        const float r1 = 1.f / sqrtf(wave_sum(vs) * (1.f / DM) + LN_EPS);
        s = 0.f;
#pragma unroll
        for (int e = 0; e < 8; ++e) { z[e] *= r1; s += z[e]; }
        const float m2 = wave_sum(s) * (1.f / DM);
        vs = 0.f;
#pragma unroll
        for (int e = 0; e < 8; ++e) { z[e] -= m2; vs = fmaf(z[e], z[e], vs); }
        const float r2 = 1.f / sqrtf(wave_sum(vs) * (1.f / DM) + LN_EPS);
#pragma unroll
        for (int e = 0; e < 8; ++e) z[e] *= r2;
      }
      const int l0 = (4 * cg) & 63;
      if (lane >= l0 && lane < l0 + 4) {
        const bool hi = cg >= 16;
        const f32x4 tz = {hi ? z[4] : z[0], hi ? z[5] : z[1], hi ? z[6] : z[2], hi ? z[7] : z[3]};
        const f32x4 th = {hi ? hv[4] : hv[0], hi ? hv[5] : hv[1], hi ? hv[6] : hv[2], hi ? hv[7] : hv[3]};
        *(LAS f32x4*)(L + L_T + (lane - l0) * 16) = tz;
        *(LAS f32x4*)(L + L_T + 64 + (lane - l0) * 16) = th;
      }
      STAMP(8);
    }
  }
  float w1v[7], b1v[7], w2v[7];
#pragma unroll
  for (int i = 0; i < 7; ++i) { w1v[i] = bf1(w1r[i]); b1v[i] = bf1(b1r[i]); w2v[i] = bf1(w2r[i]); }
  wg_barrier();
  {
    const int eh = wave >> 2;
    const f32x4 t0 = *(LAS f32x4*)(L + L_T + eh * 32), t1 = *(LAS f32x4*)(L + L_T + eh * 32 + 16);
    constexpr float K0 = -2.f * 0.7978845608028654f * 1.4426950408889634f, K1 = K0 * 0.044715f;
    float acc[8];
#pragma unroll
    for (int e = 0; e < 8; ++e) {
      const float t = e < 4 ? t0[e] : t1[e - 4];
      float a = 0.f;
#pragma unroll
      for (int i = 0; i < 7; ++i) {
        const float u = fmaf(t, w1v[i], b1v[i]);
        const float ex = __builtin_amdgcn_exp2f(u * fmaf(u * u, K1, K0));
        const float r = __builtin_amdgcn_rcpf(1.f + ex);
        a = fmaf(u * r, w2v[i], a);
      }
      acc[e] = a;
    }
#pragma unroll
    for (int e = 0; e < 8; ++e) acc[e] = wave_sum(acc[e]);
    if (lane == 0) {
      *(LAS f32x4*)(L + L_MLP + wave * 32) = f32x4{acc[0], acc[1], acc[2], acc[3]};
      *(LAS f32x4*)(L + L_MLP + wave * 32 + 16) = f32x4{acc[4], acc[5], acc[6], acc[7]};
    }
  }
  wg_barrier();
  if (tid < 16) {
    const int eh = tid >> 3, e = tid & 7;
    float y = 0.f;
#pragma unroll
    for (int w = 0; w < 4; ++w) y += *(LAS float*)(L + L_MLP + (eh * 4 + w) * 32 + e * 4);
    const float h = *(LAS float*)(L + L_T + 64 + tid * 4);
    const float o = h + (y + bf1(b2r));
    p.out[b * DM + cg * 16 + tid] = (unsigned short)(pk_bf16(o, o) & 0xffffu);
  }
  if (wave == 0) STAMP(9);
}

}

extern "C" void kernel_launch(void* const* d_in, const int* in_sizes, int n_in, void* d_out, int out_size, void* d_ws, size_t ws_size,
                              hipStream_t stream) {
  Params p;
  p.x = (const unsigned short*)d_in[0]; p.k = (const unsigned short*)d_in[1]; p.v = (const unsigned short*)d_in[2];
  p.wq = (const unsigned short*)d_in[3]; p.wo = (const unsigned short*)d_in[4];
  p.w1 = (const unsigned short*)d_in[6]; p.b1 = (const unsigned short*)d_in[7]; p.w2 = (const unsigned short*)d_in[8]; p.b2 = (const unsigned short*)d_in[9];
  p.out = (unsigned short*)d_out; p.ws = (unsigned char*)d_ws;
  (void)hipFuncSetAttribute((const void*)fused_decode_kernel, hipFuncAttributeMaxDynamicSharedMemorySize, (int)L_END);
  hipLaunchKernelGGL(fused_decode_kernel, dim3(NWG), dim3(NTHR), L_END, stream, p);
}
```

```cpp
#define X_EARLY 1
#define X_WONT 0
#define WQ_NT 0
#define FLY_PIECES 2
#define X_H1 4
#define X_H2 32
#include <hip/hip_runtime.h>
#include <hip/hip_bf16.h>
#include <stdint.h>

#define LAS __attribute__((address_space(3)))
#define GAS __attribute__((address_space(1)))
typedef unsigned v4u __attribute__((ext_vector_type(4)));
typedef unsigned v2u __attribute__((ext_vector_type(2)));
typedef int v4i __attribute__((ext_vector_type(4)));
typedef float f32x4 __attribute__((ext_vector_type(4)));
typedef float f32x2 __attribute__((ext_vector_type(2)));
typedef short s16x4 __attribute__((ext_vector_type(4)));
typedef __bf16 bf16x8 __attribute__((ext_vector_type(8)));
typedef __bf16 bf16x2 __attribute__((ext_vector_type(2)));
typedef unsigned long long u64;

#define NT_RING 4
#define X_NOWQ 0
#define X_NOGATHER 0
#define X_QSLEEP 1
#define X_LATEMISC 0
#define X_MAP 1
#define X_KSPLIT 0
#define X_NOROT 0
#define X_NOSWZ 0
#define X_NOWO 0
#define STAMPS 0

namespace {

constexpr int NB = 8, DM = 512, HQ = 32, HKV = 8, SK = 8192, DH = 128, FF = 1792;
constexpr int NWG = 256, NTHR = 512;
constexpr int NT = NT_RING;
constexpr int TILES_PER_WAVE = 32;
static_assert(NT == 4, "ring = 8 pieces");
constexpr int FLY = FLY_PIECES;
static_assert(FLY >= 1 && FLY <= 6, "in-flight pieces");
constexpr float LN_EPS = 1e-5f;
constexpr float SCALE_LOG2E = 0.08838834764831845f * 1.4426950408889634f;

constexpr unsigned WS_EPOCH = 0;
constexpr unsigned WS_FLAG = 4096;
constexpr unsigned WS_Y = 8192;
constexpr unsigned WS_Q = WS_Y + NB * HKV * DM * 4;
constexpr unsigned REC_BYTES = 2304;
constexpr unsigned WS_REC = WS_Q + NB * 4096 * 8;
constexpr unsigned WS_STAMP = WS_REC + NWG * REC_BYTES;
constexpr unsigned WS_END = WS_STAMP + NWG * 16 * 8;

constexpr unsigned L_RING = 0;
constexpr unsigned L_STAGE = 4 * NT * 8192;
constexpr unsigned L_QBUF = L_STAGE + 16384;
constexpr unsigned L_PART = L_QBUF + 1024;
constexpr unsigned L_ML = L_PART + 8192;
constexpr unsigned L_STAT = L_ML + 128;
constexpr unsigned L_Y = L_STAT + 64;
constexpr unsigned L_T = L_Y + 2048;
constexpr unsigned L_MLP = L_T + 128;
constexpr unsigned L_CTL = L_MLP + 512;
constexpr unsigned L_END = L_CTL + 128;
static_assert(L_END <= 160 * 1024, "LDS budget");
enum { C_QRDY = 0, C_STAT = 1, C_MERGE = 2, C_YCNT = 3, C_STGFREE = 4, C_TLO = 6, C_THI = 7, C_TRDY = 8, C_NEAR = 9, C_MID = 10, C_FLAGOK = 11 };

constexpr unsigned SPIN_MAX = 1u << 18;

struct Params {
  const unsigned short* x; const unsigned short* k; const unsigned short* v; const unsigned short* wq; const unsigned short* wo;
  const unsigned short* w1; const unsigned short* b1; const unsigned short* w2; const unsigned short* b2;
  unsigned short* out; unsigned char* ws;
};

__device__ __forceinline__ v4i mkrsrc(const void* p, unsigned bytes) {
  const u64 a = (u64)p;
  v4i r; r.x = __builtin_amdgcn_readfirstlane((int)(unsigned)a); r.y = __builtin_amdgcn_readfirstlane((int)((unsigned)(a >> 32) & 0xffffu));
  r.z = (int)bytes; r.w = 0x00020000; return r;
}
__device__ __forceinline__ void dma4_nt(v4i rs, unsigned v0, unsigned v1, unsigned v2, unsigned v3, unsigned soff, unsigned lds) {
  asm volatile("s_mov_b32 m0, %6\n\ts_nop 0\n\t"
               "buffer_load_dwordx4 %0, %4, %5 offen nt lds\n\t"
               "buffer_load_dwordx4 %1, %4, %5 offen offset:1024 nt lds\n\t"
               "buffer_load_dwordx4 %2, %4, %5 offen offset:2048 nt lds\n\t"
               "buffer_load_dwordx4 %3, %4, %5 offen offset:3072 nt lds"
               :: "v"(v0), "v"(v1), "v"(v2), "v"(v3), "s"(rs), "s"(soff), "s"(lds) : "memory");
}
template <int NTP> __device__ __forceinline__ void dma1(v4i rs, unsigned voff, unsigned soff, unsigned lds) {
#define DMA1(POL) asm volatile("s_mov_b32 m0, %3\n\ts_nop 0\n\tbuffer_load_dwordx4 %0, %1, %2 offen " POL " lds" :: "v"(voff), "s"(rs), "s"(soff), "s"(lds) : "memory")
  if (NTP == 0) DMA1(""); else if (NTP == 1) DMA1("nt"); else if (NTP == 2) DMA1("sc1"); else if (NTP == 3) DMA1("sc0 sc1"); else if (NTP == 4) DMA1("sc1 nt"); else DMA1("sc0");
}
template <int N> __device__ __forceinline__ void wait_vm() { asm volatile("s_waitcnt vmcnt(%0)" :: "i"(N) : "memory"); }
__device__ __forceinline__ void wait_lgkm() { asm volatile("s_waitcnt lgkmcnt(0)" ::: "memory"); }
__device__ __forceinline__ void wg_barrier() { asm volatile("s_waitcnt lgkmcnt(0)\n\ts_barrier" ::: "memory"); }

template <int CTRL> __device__ __forceinline__ float dpp(float x) {
  return __builtin_bit_cast(float, __builtin_amdgcn_mov_dpp(__builtin_bit_cast(int, x), CTRL, 0xf, 0xf, true));
}
constexpr int DPP_XOR1 = 0xB1, DPP_XOR2 = 0x4E, DPP_HMIRROR = 0x141, DPP_ROR8 = 0x128, DPP_ROR4 = 0x124;
__device__ __forceinline__ float xrow_max(float x) {
  auto s = __builtin_amdgcn_permlane16_swap(__float_as_uint(x), __float_as_uint(x), false, false);
  x = fmaxf(__uint_as_float(s[0]), __uint_as_float(s[1]));
  auto t = __builtin_amdgcn_permlane32_swap(__float_as_uint(x), __float_as_uint(x), false, false);
  return fmaxf(__uint_as_float(t[0]), __uint_as_float(t[1]));
}
__device__ __forceinline__ float xrow_sum(float x) {
  auto s = __builtin_amdgcn_permlane16_swap(__float_as_uint(x), __float_as_uint(x), false, false);
  x = __uint_as_float(s[0]) + __uint_as_float(s[1]);
  auto t = __builtin_amdgcn_permlane32_swap(__float_as_uint(x), __float_as_uint(x), false, false);
  return __uint_as_float(t[0]) + __uint_as_float(t[1]);
}
__device__ __forceinline__ float row16_sum(float x) {
  x += dpp<DPP_XOR1>(x); x += dpp<DPP_XOR2>(x); x += dpp<DPP_HMIRROR>(x); x += dpp<DPP_ROR8>(x); return x;
}
__device__ __forceinline__ float wave_sum(float x) { return xrow_sum(row16_sum(x)); }
__device__ __forceinline__ unsigned pk_bf16(float lo, float hi) {
  bf16x2 r; r[0] = (__bf16)lo; r[1] = (__bf16)hi; return __builtin_bit_cast(unsigned, r);
}
__device__ __forceinline__ float bf_lo(unsigned u) { return __uint_as_float(u << 16); }
__device__ __forceinline__ float bf_hi(unsigned u) { return __uint_as_float(u & 0xffff0000u); }
__device__ __forceinline__ float bf1(unsigned short u) { return __uint_as_float((unsigned)u << 16); }
__device__ __forceinline__ f32x4 mfma32(v4u a, v4u b, f32x4 c) {
  return __builtin_amdgcn_mfma_f32_16x16x32_bf16(__builtin_bit_cast(bf16x8, a), __builtin_bit_cast(bf16x8, b), c, 0, 0, 0);
}
__device__ __forceinline__ f32x4 mfma16(v2u a, v2u b, f32x4 c) {
  return __builtin_amdgcn_mfma_f32_16x16x16bf16_1k(__builtin_bit_cast(s16x4, a), __builtin_bit_cast(s16x4, b), c, 0, 0, 0);
}
__device__ __forceinline__ v2u tr_read(LAS unsigned char* p) {
  return __builtin_bit_cast(v2u, __builtin_amdgcn_ds_read_tr16_b64_v4i16((LAS s16x4*)p));
}
__device__ __forceinline__ v4u ld_sc1(__amdgpu_buffer_rsrc_t rs, unsigned off) { return __builtin_amdgcn_raw_buffer_load_b128(rs, (int)off, 0, 16); }
__device__ __forceinline__ void st_sc1(__amdgpu_buffer_rsrc_t rs, unsigned off, v4u v) { __builtin_amdgcn_raw_buffer_store_b128(v, rs, (int)off, 0, 16); }
__device__ __forceinline__ unsigned lds_ld(LAS unsigned char* L, int idx) { return *(volatile LAS unsigned*)(L + L_CTL + 4 * idx); }
__device__ __forceinline__ void lds_st(LAS unsigned char* L, int idx, unsigned v) { *(volatile LAS unsigned*)(L + L_CTL + 4 * idx) = v; }
__device__ __forceinline__ void lds_wait(LAS unsigned char* L, int idx, unsigned want) {
  for (unsigned s = 0; s < SPIN_MAX; ++s) { if (lds_ld(L, idx) >= want) break; __builtin_amdgcn_s_sleep(1); }
  asm volatile("" ::: "memory");
}
__device__ __forceinline__ unsigned tagf(float f) { return __float_as_uint(f) | 1u; }
#define STAMP(i) do { } while (0)

struct AttnState { float m, l; f32x4 o[8]; };

struct StreamCtx {
  LAS unsigned char* L; unsigned ring; v4i rk, rv; unsigned vk[4], vv[2], ka[4], va[8]; unsigned gbase; int sp, wave;
  __device__ __forceinline__ unsigned toff(int j) const {
    return gbase + (unsigned)(16 * j + 4 * ((sp + (j >> 2)) & 3) + ((wave + j) & 3)) * 4096u;
  }
  template <int KIND> __device__ __forceinline__ void issue(int j, int slot) const {
    if (KIND == 0) dma4_nt(rk, vk[0], vk[1], vk[2], vk[3], toff(j), ring + slot * 4096);
    else dma4_nt(rv, vv[0], vv[1], vv[0], vv[1], toff(j), ring + slot * 4096);
  }
};
constexpr int NPIECE = 2 * TILES_PER_WAVE;
constexpr int AHEAD = 16;
constexpr int wait_count(int n) {
  int last = n + FLY - 1;
  if (n >= 8 && last < AHEAD - 1) last = AHEAD - 1;
  if (last > NPIECE - 1) last = NPIECE - 1;
  return 4 * (last - n);
}
constexpr bool do_issue(int n) { return (n < 8 || n + FLY >= AHEAD) && n + FLY < NPIECE; }

__device__ __forceinline__ void softmax_pv(AttnState& st, const f32x4 sc, const v2u (&vf)[8], bool lds_pending) {
  float s0 = sc[0] * SCALE_LOG2E, s1 = sc[1] * SCALE_LOG2E, s2 = sc[2] * SCALE_LOG2E, s3 = sc[3] * SCALE_LOG2E;
  float tm = xrow_max(fmaxf(fmaxf(s0, s1), fmaxf(s2, s3)));
  if (__builtin_amdgcn_ballot_w64(tm > st.m) != 0ull) {
    const float mn = fmaxf(st.m, tm);
    const float al = __builtin_amdgcn_exp2f(st.m - mn);
    st.m = mn; st.l *= al;
    const float a0 = dpp<0x00>(al), a1 = dpp<0x55>(al), a2 = dpp<0xAA>(al), a3 = dpp<0xFF>(al);
#pragma unroll
    for (int c = 0; c < 8; ++c) { st.o[c][0] *= a0; st.o[c][1] *= a1; st.o[c][2] *= a2; st.o[c][3] *= a3; }
  }
  const float p0 = __builtin_amdgcn_exp2f(s0 - st.m), p1 = __builtin_amdgcn_exp2f(s1 - st.m);
  const float p2 = __builtin_amdgcn_exp2f(s2 - st.m), p3 = __builtin_amdgcn_exp2f(s3 - st.m);
  st.l += (p0 + p1) + (p2 + p3);
  v2u pf; pf.x = pk_bf16(p0, p1); pf.y = pk_bf16(p2, p3);
  if (lds_pending) wait_lgkm();
#pragma unroll
  for (int c = 0; c < 8; ++c) st.o[c] = mfma16(pf, vf[c], st.o[c]);
}
template <int TC>
__device__ __forceinline__ void attn_tile(AttnState& st, const StreamCtx& cx, const v4u (&qf)[4], int t) {
  constexpr int n0 = 2 * TC, n1 = 2 * TC + 1;
  LAS unsigned char* kb = cx.L + cx.ring + (n0 & 7) * 4096;
  wait_vm<wait_count(n0)>();
  if constexpr (do_issue(n0)) cx.issue<(n0 + FLY) & 1>(t + (FLY >> 1), (n0 + FLY) & 7);
  v4u kf[4];
#pragma unroll
  for (int s = 0; s < 4; ++s) kf[s] = *(LAS v4u*)(kb + cx.ka[s]);
  f32x4 sc = {0.f, 0.f, 0.f, 0.f};
#pragma unroll
  for (int s = 0; s < 4; ++s) sc = mfma32(kf[s], qf[s], sc);
  wait_vm<wait_count(n1)>();
  if constexpr (do_issue(n1)) cx.issue<(n1 + FLY) & 1>(t + ((FLY + 1) >> 1), (n1 + FLY) & 7);
  v2u vf[8];
#pragma unroll
  for (int c = 0; c < 8; ++c) vf[c] = tr_read(kb + 4096 + cx.va[c]);
  softmax_pv(st, sc, vf, true);
}
struct ParkedTile { v4u kf[4]; v2u vf[8]; };
template <int TC>
__device__ __forceinline__ void park_tile(ParkedTile& pt, const StreamCtx& cx) {
  constexpr int n0 = 2 * TC, n1 = 2 * TC + 1;
  LAS unsigned char* kb = cx.L + cx.ring + (n0 & 7) * 4096;
  wait_vm<wait_count(n0)>();
  cx.issue<(n0 + FLY) & 1>(TC + (FLY >> 1), (n0 + FLY) & 7);
#pragma unroll
  for (int s = 0; s < 4; ++s) pt.kf[s] = *(LAS v4u*)(kb + cx.ka[s]);
  wait_vm<wait_count(n1)>();
  cx.issue<(n1 + FLY) & 1>(TC + ((FLY + 1) >> 1), (n1 + FLY) & 7);
#pragma unroll
  for (int c = 0; c < 8; ++c) pt.vf[c] = tr_read(kb + 4096 + cx.va[c]);
  wait_lgkm();
}
__device__ __forceinline__ void parked_compute(AttnState& st, const ParkedTile& pt, const v4u (&qf)[4]) {
  f32x4 sc = {0.f, 0.f, 0.f, 0.f};
#pragma unroll
  for (int s = 0; s < 4; ++s) sc = mfma32(pt.kf[s], qf[s], sc);
  softmax_pv(st, sc, pt.vf, false);
}
template <int N> __device__ __forceinline__ void first_issue(const StreamCtx& cx) {
  cx.issue<N & 1>(N >> 1, N);
  if constexpr (N + 1 < FLY) first_issue<N + 1>(cx);
}
template <int N> __device__ __forceinline__ void ahead_issue(const StreamCtx& cx) {
  if constexpr (N < AHEAD) {
    wait_vm<4 * (FLY - 1)>();
    cx.issue<N & 1>(N >> 1, N & 7);
    ahead_issue<N + 1>(cx);
  }
}

__global__ void __launch_bounds__(NTHR, 2) fused_decode_kernel(Params p) {
  extern __shared__ __attribute__((aligned(16))) unsigned char lds_raw[];
  LAS unsigned char* L = (LAS unsigned char*)lds_raw;
  const int tid = threadIdx.x, lane = tid & 63, wave = __builtin_amdgcn_readfirstlane(tid >> 6);
  const int c = blockIdx.x;
  const int b = c >> 5, kvh = c & 7, sp = (c >> 3) & 3, grp = b * 8 + kvh;
  const int rid = grp * 4 + sp;
  const int cq = (c & 7) * 32 + (c >> 3);
  const int e16 = c & 31;
  const int g = lane >> 4, c16 = lane & 15;
  const __amdgpu_buffer_rsrc_t rws = __builtin_amdgcn_make_buffer_rsrc(p.ws, 0, WS_END, 0x00020000);

  float k3[7], k2[7], k1[7], k0[7], wa[7], wb[7]; unsigned short b2r;
  auto load_params = [&]() {
    constexpr float K0 = -2.f * 0.7978845608028654f * 1.4426950408889634f, K1 = K0 * 0.044715f;
    unsigned short w1r[7], b1r[7], w2r[7];
    const int f0 = tid & 255;
#pragma unroll
    for (int i = 0; i < 7; ++i) { w1r[i] = p.w1[f0 + 256 * i]; b1r[i] = p.b1[f0 + 256 * i]; w2r[i] = p.w2[f0 + 256 * i]; }
    b2r = p.b2[0];
#pragma unroll
    for (int i = 0; i < 7; ++i) {
      const float a = bf1(w1r[i]), bb = bf1(b1r[i]), w2 = bf1(w2r[i]);
      k3[i] = K1 * a * a * a; k2[i] = 3.f * K1 * a * a * bb; k1[i] = a * fmaf(3.f * K1 * bb, bb, K0); k0[i] = bb * fmaf(K1 * bb, bb, K0);
      wa[i] = w2 * a; wb[i] = w2 * bb;
    }
  };

  if (wave < 4) {
    if (wave == 0) STAMP(0);
    StreamCtx cx;
    cx.L = L; cx.rk = mkrsrc(p.k, 1u << 27); cx.rv = mkrsrc(p.v, 1u << 27); cx.sp = sp; cx.wave = wave;
#pragma unroll
    for (int i = 0; i < 4; ++i) cx.vk[i] = X_NOSWZ ? lane * 16 : g * 256 + 16 * (c16 ^ (4 * i + g));
#pragma unroll
    for (int i = 0; i < 2; ++i) cx.vv[i] = X_NOSWZ ? lane * 16 : g * 256 + 16 * (c16 ^ (((4 * i + g) & 7) << 1));
#pragma unroll
    for (int s = 0; s < 4; ++s) cx.ka[s] = 256 * c16 + 16 * ((4 * s + g) ^ c16);
    {
      const int q4 = c16 >> 2, p4 = lane & 3, row = 4 * g + q4;
#pragma unroll
      for (int cc = 0; cc < 8; ++cc) cx.va[cc] = 256 * row + 16 * ((2 * cc + (p4 >> 1)) ^ ((row & 7) << 1)) + 8 * (p4 & 1);
    }
    cx.ring = L_RING + wave * (NT * 8192);
    cx.gbase = (unsigned)grp * (2048u * 1024u);
    first_issue<0>(cx);
    if (wave == 0) STAMP(10);
    wg_barrier();
    ParkedTile pk0, pk1, pk2, pk3;
    park_tile<0>(pk0, cx); park_tile<1>(pk1, cx); park_tile<2>(pk2, cx); park_tile<3>(pk3, cx);
    if (wave == 0) STAMP(11);
    ahead_issue<8 + FLY>(cx);
    if (wave == 0) STAMP(12);
    lds_wait(L, C_QRDY, 1u);
    v4u qf[4];
#pragma unroll
    for (int s = 0; s < 4; ++s) qf[s] = *(LAS v4u*)(L + L_QBUF + (lane & 3) * 256 + s * 64 + g * 16);
    AttnState st; st.m = -INFINITY; st.l = 0.f;
#pragma unroll
    for (int cc = 0; cc < 8; ++cc) st.o[cc] = f32x4{0.f, 0.f, 0.f, 0.f};
    attn_tile<4>(st, cx, qf, 4); attn_tile<5>(st, cx, qf, 5); attn_tile<6>(st, cx, qf, 6); attn_tile<7>(st, cx, qf, 7);
    parked_compute(st, pk0, qf); attn_tile<8>(st, cx, qf, 8);
    parked_compute(st, pk1, qf); attn_tile<9>(st, cx, qf, 9);
    parked_compute(st, pk2, qf); attn_tile<10>(st, cx, qf, 10);
    parked_compute(st, pk3, qf); attn_tile<11>(st, cx, qf, 11);
    if (wave == 0) STAMP(13);
    if (wave == 0) lds_st(L, C_MID, 1u);
    for (int j = 12; j < TILES_PER_WAVE - 4; j += 4) {
      if (wave == 0 && j == 20) STAMP(14);
      attn_tile<12>(st, cx, qf, j); attn_tile<13>(st, cx, qf, j + 1); attn_tile<14>(st, cx, qf, j + 2); attn_tile<15>(st, cx, qf, j + 3);
    }
    if (wave == 0) STAMP(15);
    attn_tile<28>(st, cx, qf, 28); attn_tile<29>(st, cx, qf, 29);
    if (wave == 0) lds_st(L, C_NEAR, 1u);
    attn_tile<30>(st, cx, qf, 30); attn_tile<31>(st, cx, qf, 31);
    if (wave == 0) STAMP(2);
    const float lsum = xrow_sum(st.l);
#pragma unroll
    for (int cc = 0; cc < 8; ++cc) {
      const float v = g == 0 ? st.o[cc][0] : g == 1 ? st.o[cc][1] : g == 2 ? st.o[cc][2] : st.o[cc][3];
      *(LAS float*)(L + L_PART + wave * 2048 + g * 512 + (16 * cc + c16) * 4) = v;
    }
    if (lane < 4) *(LAS v2u*)(L + L_ML + wave * 32 + lane * 8) = v2u{__float_as_uint(st.m), __float_as_uint(lsum)};
    wait_lgkm();
    unsigned old = 0;
    if (lane == 0) old = __hip_atomic_fetch_add((LAS unsigned*)(L + L_CTL + 4 * C_MERGE), 1u, __ATOMIC_RELAXED, __HIP_MEMORY_SCOPE_WORKGROUP);
    old = __builtin_amdgcn_readfirstlane(old);
    asm volatile("" ::: "memory");
    if (old == 3u) {
      float mw[4], lw[4];
#pragma unroll
      for (int w = 0; w < 4; ++w) { const v2u ml = *(LAS v2u*)(L + L_ML + w * 32 + g * 8); mw[w] = __uint_as_float(ml.x); lw[w] = __uint_as_float(ml.y); }
      const float M = fmaxf(fmaxf(mw[0], mw[1]), fmaxf(mw[2], mw[3]));
      float Ls = 0.f; f32x4 a0 = {0.f, 0.f, 0.f, 0.f}, a1 = {0.f, 0.f, 0.f, 0.f};
#pragma unroll
      for (int w = 0; w < 4; ++w) {
        const float f = __builtin_amdgcn_exp2f(mw[w] - M);
        const f32x4 x0 = *(LAS f32x4*)(L + L_PART + w * 2048 + g * 512 + c16 * 16), x1 = *(LAS f32x4*)(L + L_PART + w * 2048 + g * 512 + 256 + c16 * 16);
        Ls = fmaf(f, lw[w], Ls); a0 += f * x0; a1 += f * x1;
      }
      const unsigned rec = WS_REC + (unsigned)rid * REC_BYTES;
      st_sc1(rws, rec + g * 512 + c16 * 16, v4u{tagf(a0[0]), tagf(a0[1]), tagf(a0[2]), tagf(a0[3])});
      st_sc1(rws, rec + g * 512 + 256 + c16 * 16, v4u{tagf(a1[0]), tagf(a1[1]), tagf(a1[2]), tagf(a1[3])});
      if (c16 == 0) __builtin_amdgcn_raw_buffer_store_b64(v2u{tagf(M), tagf(Ls)}, rws, (int)(rec + 2048 + g * 8), 0, 16);
      STAMP(3);
    }
    load_params();
  } else {
    const int hw = wave - 4;
    if (hw == 0) {
      const v4i rq = mkrsrc(p.wq, DM * HQ * DH * 2);
      const unsigned vo = (lane >> 1) * 8192 + (lane & 1) * 16;
#pragma unroll
      for (int s = 0; s < 16; ++s) if (!X_NOWQ) {
        if (X_KSPLIT) dma1<WQ_NT>(rq, (lane >> 3) * 8192 + (lane & 7) * 16, (unsigned)(cq >> 2) * 128u + (unsigned)((cq & 3) * 128 + s * 8) * 8192u, L_STAGE + s * 1024);
        else dma1<WQ_NT>(rq, vo, (unsigned)cq * 32u + (unsigned)s * (32u * 8192u), L_STAGE + s * 1024);
      }
      v4u xf[16];
      const GAS v4u* xp = (const GAS v4u*)(p.x + (c16 & 7) * DM + g * 8);
#pragma unroll
      for (int s = 0; s < 16; ++s) xf[s] = xp[s * 4];
      const u64 ep = __hip_atomic_load((GAS u64*)(p.ws + WS_EPOCH), __ATOMIC_RELAXED, __HIP_MEMORY_SCOPE_AGENT);
      const u64 T = ep + 0x9E3779B97F4A7C15ull;
      wg_barrier();
      if (lane == 0) { lds_st(L, C_TLO, (unsigned)T); lds_st(L, C_THI, (unsigned)(T >> 32)); wait_lgkm(); lds_st(L, C_TRDY, 1u); }
      if (c16 >= 8) {
#pragma unroll
        for (int s = 0; s < 16; ++s) xf[s] = v4u{0x3f803f80u, 0x3f803f80u, 0x3f803f80u, 0x3f803f80u};
      }
      wait_vm<0>();
      STAMP(1);
      f32x4 d = {0.f, 0.f, 0.f, 0.f};
      const unsigned ta = (8 * g + (c16 >> 2)) * 32 + (lane & 3) * 8;
#pragma unroll
      for (int s = 0; s < 16; ++s) {
        const v2u lo = tr_read(L + L_STAGE + s * 1024 + ta), hi = tr_read(L + L_STAGE + s * 1024 + ta + 128);
        d = mfma32(v4u{lo.x, lo.y, hi.x, hi.y}, xf[s], d);
      }
      lds_wait(L, C_STAT, 1u);
      const float mean = *(LAS float*)(L + L_STAT + (c16 & 7) * 8), rstd = *(LAS float*)(L + L_STAT + (c16 & 7) * 8 + 4);
      unsigned gl[4];
#pragma unroll
      for (int r = 0; r < 4; ++r) {
        const float cs = dpp<DPP_ROR8>(d[r]);
        const float q = rstd * (d[r] - mean * cs);
        gl[r] = (pk_bf16(q, q) & 0xffffu) | ((unsigned)T << 16);
      }
      const unsigned thi = (unsigned)(T >> 16);
      if (c16 < 8) {
        const unsigned off = WS_Q + ((unsigned)c16 * 4096u + (unsigned)cq * 16u + g * 4u) * 8u;
        st_sc1(rws, off, v4u{gl[0], thi, gl[1], thi});
        st_sc1(rws, off + 16, v4u{gl[2], thi, gl[3], thi});
      }
      STAMP(4);
      const unsigned qoff = WS_Q + ((unsigned)b * 4096u + (unsigned)kvh * 512u) * 8u + lane * 16u;
      const unsigned tlo = (unsigned)T << 16;
      v4u a0, a1, a2, a3; unsigned bad = 1u;
      for (unsigned s = 0; s < SPIN_MAX; ++s) {
        asm volatile("" ::: "memory");
        if (bad != 0u) {
          a0 = ld_sc1(rws, qoff); a1 = ld_sc1(rws, qoff + 1024); a2 = ld_sc1(rws, qoff + 2048); a3 = ld_sc1(rws, qoff + 3072);
          bad = (((a0.x ^ tlo) | (a0.z ^ tlo) | (a1.x ^ tlo) | (a1.z ^ tlo) | (a2.x ^ tlo) | (a2.z ^ tlo) | (a3.x ^ tlo) | (a3.z ^ tlo)) >> 16) |
                (a0.y ^ thi) | (a0.w ^ thi) | (a1.y ^ thi) | (a1.w ^ thi) | (a2.y ^ thi) | (a2.w ^ thi) | (a3.y ^ thi) | (a3.w ^ thi);
        }
        if (X_NOGATHER || __builtin_amdgcn_ballot_w64(bad != 0u) == 0ull) break;
        __builtin_amdgcn_s_sleep(X_QSLEEP);
      }
      *(LAS unsigned*)(L + L_QBUF + lane * 4) = (a0.x & 0xffffu) | (a0.z << 16);
      *(LAS unsigned*)(L + L_QBUF + 256 + lane * 4) = (a1.x & 0xffffu) | (a1.z << 16);
      *(LAS unsigned*)(L + L_QBUF + 512 + lane * 4) = (a2.x & 0xffffu) | (a2.z << 16);
      *(LAS unsigned*)(L + L_QBUF + 768 + lane * 4) = (a3.x & 0xffffu) | (a3.z << 16);
      wait_lgkm();
      lds_st(L, C_QRDY, 1u);
      STAMP(5);
    } else if (hw == 1) {
      v4u xv[8];
      const GAS v4u* xp = (const GAS v4u*)(p.x + (lane >> 3) * DM) + (lane & 7);
#pragma unroll
      for (int i = 0; i < 8; ++i) xv[i] = xp[i * 8];
      wg_barrier();
      float s = 0.f;
#pragma unroll
      for (int i = 0; i < 8; ++i)
#pragma unroll
        for (int e = 0; e < 4; ++e) s += bf_lo(xv[i][e]) + bf_hi(xv[i][e]);
      s += dpp<DPP_XOR1>(s); s += dpp<DPP_XOR2>(s); s += dpp<DPP_HMIRROR>(s);
      const float mean = s * (1.f / DM);
      float vs = 0.f;
#pragma unroll
      for (int i = 0; i < 8; ++i)
#pragma unroll
        for (int e = 0; e < 4; ++e) { const float d0 = bf_lo(xv[i][e]) - mean, d1 = bf_hi(xv[i][e]) - mean; vs = fmaf(d0, d0, vs); vs = fmaf(d1, d1, vs); }
      vs += dpp<DPP_XOR1>(vs); vs += dpp<DPP_XOR2>(vs); vs += dpp<DPP_HMIRROR>(vs);
      const float rstd = 1.f / sqrtf(vs * (1.f / DM) + LN_EPS);
      if ((lane & 7) == 0) { *(LAS float*)(L + L_STAT + (lane >> 3) * 8) = mean; *(LAS float*)(L + L_STAT + (lane >> 3) * 8 + 4) = rstd; }
      wait_lgkm();
      lds_st(L, C_STAT, 1u);
    } else if (hw == 2) {
      wg_barrier();
    } else {
      if (lane < 32) lds_st(L, lane, 0u);
      wg_barrier();
    }
    lds_wait(L, C_QRDY, 1u);
    const u64 T = (u64)lds_ld(L, C_TLO) | ((u64)lds_ld(L, C_THI) << 32);
    if (X_LATEMISC) lds_wait(L, C_MID, 1u);
    const unsigned yslot = WS_Y + (((unsigned)b * 8u + (unsigned)kvh) * 512u + (unsigned)sp * 128u) * 4u;
    if (hw == 2) {
      const v4u z = {0u, 0u, 0u, 0u};
      const unsigned rec = WS_REC + (unsigned)rid * REC_BYTES;
      st_sc1(rws, rec + lane * 16, z); st_sc1(rws, rec + 1024 + lane * 16, z);
      if (lane < 2) st_sc1(rws, rec + 2048 + lane * 16, z);
      if (lane < 32) st_sc1(rws, yslot + lane * 16, z);
      wait_vm<0>();
      if (lane == 0) __hip_atomic_store((GAS u64*)(p.ws + WS_FLAG) + c, T, __ATOMIC_RELAXED, __HIP_MEMORY_SCOPE_AGENT);
    }
    v2u wf[8][8];
    {
      const v4i ro = mkrsrc(p.wo, HQ * DH * DM * 2);
      const unsigned stg = L_STAGE + hw * 4096;
      const int q4 = c16 >> 2, p4 = lane & 3, row = 4 * g + q4;
      const unsigned vo0 = g * 1024 + 16 * (c16 ^ ((g & 7) << 1)), vo1 = g * 1024 + 16 * (c16 ^ (((4 + g) & 7) << 1));
      const unsigned ra = 256 * row + 16 * ((p4 >> 1) ^ ((row & 7) << 1)) + 8 * (p4 & 1);
#pragma unroll
      for (int ch = 0; ch < 8; ++ch) {
        const unsigned so = (unsigned)(kvh * 512 + hw * 128 + ch * 16) * 1024u + (unsigned)sp * 256u;
#pragma unroll
        for (int i = 0; i < 4; ++i) if (!X_NOWO) dma1<X_WONT>(ro, (i & 1) ? vo1 : vo0, so + i * 4096u, stg + i * 1024);
        wait_vm<0>();
#pragma unroll
        for (int cc = 0; cc < 8; ++cc) wf[ch][cc] = tr_read(L + stg + (ra ^ (unsigned)(cc << 5)));
        wait_lgkm();
      }
    }
    load_params();
    v4u xr0, xr1;
    { const v2u t0 = *(const GAS v2u*)(p.x + b * DM + lane * 4), t1 = *(const GAS v2u*)(p.x + b * DM + 256 + lane * 4);
      xr0 = v4u{t0.x << 16, t0.x & 0xffff0000u, t0.y << 16, t0.y & 0xffff0000u}; xr1 = v4u{t1.x << 16, t1.x & 0xffff0000u, t1.y << 16, t1.y & 0xffff0000u}; }
    if (hw == 3) {
      lds_wait(L, C_MID, 1u);
      const GAS u64* fp = (const GAS u64*)(p.ws + WS_FLAG) + b * 32 + (lane & 31);
      for (unsigned s = 0; s < SPIN_MAX; ++s) {
        const u64 f = __hip_atomic_load(fp, __ATOMIC_RELAXED, __HIP_MEMORY_SCOPE_AGENT);
        if (__builtin_amdgcn_ballot_w64(f != T) == 0ull) break;
        __builtin_amdgcn_s_sleep(64);
      }
      asm volatile("" ::: "memory");
      lds_st(L, C_FLAGOK, 1u);
    }
    lds_wait(L, C_FLAGOK, 1u);
    if (hw == 0) STAMP(6);
    lds_wait(L, X_H1 ? C_MERGE : C_NEAR, X_H1 ? (unsigned)X_H1 : 1u);
    {
      const int sg = lane & 3, jq = (lane >> 2) & 3;
      const unsigned rec = WS_REC + (unsigned)(grp * 4 + sg) * REC_BYTES;
      const unsigned doff = rec + hw * 512 + (32 * jq + 4 * g) * 4;
      v4u d0, d1; v2u hd; unsigned ok = 0u;
      for (unsigned s = 0; s < SPIN_MAX; ++s) {
        asm volatile("" ::: "memory");
        if ((ok & 1u) == 0u) {
          hd = __builtin_amdgcn_raw_buffer_load_b64(rws, (int)(rec + 2048 + hw * 8), 0, 16);
          d0 = ld_sc1(rws, doff); d1 = ld_sc1(rws, doff + 64);
          ok = hd.x & hd.y & d0.x & d0.y & d0.z & d0.w & d1.x & d1.y & d1.z & d1.w;
        }
        if (__builtin_amdgcn_ballot_w64((ok & 1u) == 0u) == 0ull) break;
        __builtin_amdgcn_s_sleep(1);
      }
      const float mh = __uint_as_float(hd.x), lh = __uint_as_float(hd.y);
      float mx = fmaxf(mh, dpp<DPP_XOR1>(mh)); mx = fmaxf(mx, dpp<DPP_XOR2>(mx));
      const float e = __builtin_amdgcn_exp2f(mh - mx);
      float lt = e * lh; lt += dpp<DPP_XOR1>(lt); lt += dpp<DPP_XOR2>(lt);
      const float w = e / lt;
      const v4u af = {pk_bf16(__uint_as_float(d0.x) * w, __uint_as_float(d0.y) * w), pk_bf16(__uint_as_float(d0.z) * w, __uint_as_float(d0.w) * w),
                      pk_bf16(__uint_as_float(d1.x) * w, __uint_as_float(d1.y) * w), pk_bf16(__uint_as_float(d1.z) * w, __uint_as_float(d1.w) * w)};
      f32x4 acc[8];
#pragma unroll
      for (int cc = 0; cc < 8; ++cc) acc[cc] = f32x4{0.f, 0.f, 0.f, 0.f};
#pragma unroll
      for (int j = 0; j < 4; ++j) {
        const v4u aj = jq == j ? af : v4u{0u, 0u, 0u, 0u};
#pragma unroll
        for (int cc = 0; cc < 8; ++cc) acc[cc] = mfma32(aj, v4u{wf[2 * j][cc].x, wf[2 * j][cc].y, wf[2 * j + 1][cc].x, wf[2 * j + 1][cc].y}, acc[cc]);
      }
      float sv[8];
#pragma unroll
      for (int cc = 0; cc < 8; ++cc) sv[cc] = (acc[cc][0] + acc[cc][1]) + (acc[cc][2] + acc[cc][3]);
      float r2[2];
#pragma unroll
      for (int hh = 0; hh < 2; ++hh) {
        auto s01 = __builtin_amdgcn_permlane16_swap(__float_as_uint(sv[4 * hh]), __float_as_uint(sv[4 * hh + 1]), false, false);
        auto s23 = __builtin_amdgcn_permlane16_swap(__float_as_uint(sv[4 * hh + 2]), __float_as_uint(sv[4 * hh + 3]), false, false);
        const float x = __uint_as_float(s01[0]) + __uint_as_float(s01[1]), y = __uint_as_float(s23[0]) + __uint_as_float(s23[1]);
        auto t = __builtin_amdgcn_permlane32_swap(__float_as_uint(x), __float_as_uint(y), false, false);
        r2[hh] = __uint_as_float(t[0]) + __uint_as_float(t[1]);
      }
      *(LAS float*)(L + L_Y + hw * 512 + lane * 4) = r2[0];
      *(LAS float*)(L + L_Y + hw * 512 + 256 + lane * 4) = r2[1];
    }
    wait_lgkm();
    unsigned old = 0;
    if (lane == 0) old = __hip_atomic_fetch_add((LAS unsigned*)(L + L_CTL + 4 * C_YCNT), 1u, __ATOMIC_RELAXED, __HIP_MEMORY_SCOPE_WORKGROUP);
    old = __builtin_amdgcn_readfirstlane(old);
    asm volatile("" ::: "memory");
    if (old == 3u) {
      STAMP(7);
      if (lane < 32) {
        const f32x4 y0 = *(LAS f32x4*)(L + L_Y + lane * 16), y1 = *(LAS f32x4*)(L + L_Y + 512 + lane * 16);
        const f32x4 y2 = *(LAS f32x4*)(L + L_Y + 1024 + lane * 16), y3 = *(LAS f32x4*)(L + L_Y + 1536 + lane * 16);
        const f32x4 y = (y0 + y1) + (y2 + y3);
        st_sc1(rws, yslot + lane * 16, v4u{tagf(y[0]), tagf(y[1]), tagf(y[2]), tagf(y[3])});
      }
      const unsigned yoff = WS_Y + (unsigned)b * 16384u + lane * 16;
      {
        const unsigned ioff = WS_Y + (unsigned)b * 16384u + (lane & 31) * 512u + 508u;
        unsigned iv = 0u;
        for (unsigned s = 0; s < SPIN_MAX; ++s) {
          asm volatile("" ::: "memory");
          if ((iv & 1u) == 0u) iv = __builtin_amdgcn_raw_buffer_load_b32(rws, (int)ioff, 0, 16);
          if (__builtin_popcount((unsigned)__builtin_amdgcn_ballot_w64((iv & 1u) == 0u)) <= X_H2) break;
          __builtin_amdgcn_s_sleep(4);
        }
      }
      v4u yv[16]; unsigned pend = 0xffffu;
      for (unsigned s = 0; s < SPIN_MAX; ++s) {
        asm volatile("" ::: "memory");
#pragma unroll
        for (int i = 0; i < 16; ++i) if ((pend >> i) & 1u) yv[i] = ld_sc1(rws, yoff + i * 1024);
#pragma unroll
        for (int i = 0; i < 16; ++i) if ((pend >> i) & 1u) {
          const unsigned ok = yv[i].x & yv[i].y & yv[i].z & yv[i].w;
          if (__builtin_amdgcn_ballot_w64((ok & 1u) == 0u) == 0ull) pend &= ~(1u << i);
        }
        if (pend == 0u) break;
      }
      if (c == 0 && lane == 0) __hip_atomic_store((GAS u64*)(p.ws + WS_EPOCH), T, __ATOMIC_RELAXED, __HIP_MEMORY_SCOPE_AGENT);
      float hv[8];
#pragma unroll
      for (int e = 0; e < 4; ++e) {
        float s0 = 0.f, s1 = 0.f;
#pragma unroll
        for (int kk = 0; kk < 8; ++kk) { s0 += __uint_as_float(yv[2 * kk][e]); s1 += __uint_as_float(yv[2 * kk + 1][e]); }
        hv[e] = __uint_as_float(xr0[e]) + s0; hv[4 + e] = __uint_as_float(xr1[e]) + s1;
      }
      float z[8];
      {
        float s = 0.f;
#pragma unroll
        for (int e = 0; e < 8; ++e) s += hv[e];
        const float m1 = wave_sum(s) * (1.f / DM);
        float vs = 0.f;
#pragma unroll
        for (int e = 0; e < 8; ++e) { z[e] = hv[e] - m1; vs = fmaf(z[e], z[e], vs); }
        const float r1 = 1.f / sqrtf(wave_sum(vs) * (1.f / DM) + LN_EPS);
        s = 0.f;
#pragma unroll
        for (int e = 0; e < 8; ++e) { z[e] *= r1; s += z[e]; }
        const float m2 = wave_sum(s) * (1.f / DM);
        vs = 0.f;
#pragma unroll
        for (int e = 0; e < 8; ++e) { z[e] -= m2; vs = fmaf(z[e], z[e], vs); }
        const float r2 = 1.f / sqrtf(wave_sum(vs) * (1.f / DM) + LN_EPS);
#pragma unroll
        for (int e = 0; e < 8; ++e) z[e] *= r2;
      }
      const int l0 = (4 * e16) & 63;
      if (lane >= l0 && lane < l0 + 4) {
        const bool hi = e16 >= 16;
        const f32x4 tz = {hi ? z[4] : z[0], hi ? z[5] : z[1], hi ? z[6] : z[2], hi ? z[7] : z[3]};
        const f32x4 th = {hi ? hv[4] : hv[0], hi ? hv[5] : hv[1], hi ? hv[6] : hv[2], hi ? hv[7] : hv[3]};
        *(LAS f32x4*)(L + L_T + (lane - l0) * 16) = tz;
        *(LAS f32x4*)(L + L_T + 64 + (lane - l0) * 16) = th;
      }
      STAMP(8);
    }
  }
  wg_barrier();
  {
    const int eh = wave >> 2;
    const f32x4 t0 = *(LAS f32x4*)(L + L_T + eh * 32), t1 = *(LAS f32x4*)(L + L_T + eh * 32 + 16);
    const f32x2 tp[4] = {f32x2{t0[0], t0[1]}, f32x2{t0[2], t0[3]}, f32x2{t1[0], t1[1]}, f32x2{t1[2], t1[3]}};
    f32x2 acc[4];
#pragma unroll
    for (int e = 0; e < 4; ++e) acc[e] = f32x2{0.f, 0.f};
#pragma unroll
    for (int i = 0; i < 7; ++i) {
#pragma unroll
      for (int e = 0; e < 4; ++e) {
        const f32x2 t = tp[e];
        f32x2 ar = __builtin_elementwise_fma(f32x2{k3[i], k3[i]}, t, f32x2{k2[i], k2[i]});
        ar = __builtin_elementwise_fma(ar, t, f32x2{k1[i], k1[i]});
        ar = __builtin_elementwise_fma(ar, t, f32x2{k0[i], k0[i]});
        const f32x2 wu = __builtin_elementwise_fma(f32x2{wa[i], wa[i]}, t, f32x2{wb[i], wb[i]});
        f32x2 d = {__builtin_amdgcn_exp2f(ar.x), __builtin_amdgcn_exp2f(ar.y)};
        d += f32x2{1.f, 1.f};
        const f32x2 r = {__builtin_amdgcn_rcpf(d.x), __builtin_amdgcn_rcpf(d.y)};
        acc[e] = __builtin_elementwise_fma(wu, r, acc[e]);
      }
    }
    float r2[2];
#pragma unroll
    for (int hh = 0; hh < 2; ++hh) {
      auto s01 = __builtin_amdgcn_permlane16_swap(__float_as_uint(acc[2 * hh].x), __float_as_uint(acc[2 * hh].y), false, false);
      auto s23 = __builtin_amdgcn_permlane16_swap(__float_as_uint(acc[2 * hh + 1].x), __float_as_uint(acc[2 * hh + 1].y), false, false);
      const float x = __uint_as_float(s01[0]) + __uint_as_float(s01[1]), y = __uint_as_float(s23[0]) + __uint_as_float(s23[1]);
      auto t = __builtin_amdgcn_permlane32_swap(__float_as_uint(x), __float_as_uint(y), false, false);
      r2[hh] = row16_sum(__uint_as_float(t[0]) + __uint_as_float(t[1]));
    }
    if (c16 == 0) { *(LAS float*)(L + L_MLP + wave * 32 + g * 4) = r2[0]; *(LAS float*)(L + L_MLP + wave * 32 + 16 + g * 4) = r2[1]; }
  }
  wg_barrier();
  if (tid < 16) {
    const int eh = tid >> 3, e = tid & 7;
    float y = 0.f;
#pragma unroll
    for (int w = 0; w < 4; ++w) y += *(LAS float*)(L + L_MLP + (eh * 4 + w) * 32 + e * 4);
    const float h = *(LAS float*)(L + L_T + 64 + tid * 4);
    const float o = h + (y + bf1(b2r));
    p.out[b * DM + e16 * 16 + tid] = (unsigned short)(pk_bf16(o, o) & 0xffffu);
  }
  if (wave == 0) STAMP(9);
}

}

extern "C" void kernel_launch(void* const* d_in, const int* in_sizes, int n_in, void* d_out, int out_size, void* d_ws, size_t ws_size,
                              hipStream_t stream) {
  Params p;
  p.x = (const unsigned short*)d_in[0]; p.k = (const unsigned short*)d_in[1]; p.v = (const unsigned short*)d_in[2];
  p.wq = (const unsigned short*)d_in[3]; p.wo = (const unsigned short*)d_in[4];
  p.w1 = (const unsigned short*)d_in[6]; p.b1 = (const unsigned short*)d_in[7]; p.w2 = (const unsigned short*)d_in[8]; p.b2 = (const unsigned short*)d_in[9];
  p.out = (unsigned short*)d_out; p.ws = (unsigned char*)d_ws;
  (void)hipFuncSetAttribute((const void*)fused_decode_kernel, hipFuncAttributeMaxDynamicSharedMemorySize, (int)L_END + 128);
  hipLaunchKernelGGL(fused_decode_kernel, dim3(NWG), dim3(NTHR), L_END + 128, stream, p);
}
```

```cpp
#define STAMPS 1
#define X_EARLY 1
#define X_WONT 0
#define WQ_NT 0
#define FLY_PIECES 2
#define X_H1 4
#define X_H2 32
#include <hip/hip_runtime.h>
#include <hip/hip_bf16.h>
#include <stdint.h>
#include <stdio.h>

#define LAS __attribute__((address_space(3)))
#define GAS __attribute__((address_space(1)))
typedef unsigned v4u __attribute__((ext_vector_type(4)));
typedef unsigned v2u __attribute__((ext_vector_type(2)));
typedef int v4i __attribute__((ext_vector_type(4)));
typedef float f32x4 __attribute__((ext_vector_type(4)));
typedef float f32x2 __attribute__((ext_vector_type(2)));
typedef short s16x4 __attribute__((ext_vector_type(4)));
typedef __bf16 bf16x8 __attribute__((ext_vector_type(8)));
typedef __bf16 bf16x2 __attribute__((ext_vector_type(2)));
typedef unsigned long long u64;

#define NT_RING 4
#define X_NOWQ 0
#define X_NOGATHER 0
#define X_QSLEEP 1
#define X_LATEMISC 0
#define X_MAP 1
#define X_KSPLIT 0
#define X_NOROT 0
#define X_NOSWZ 0
#define X_NOWO 0

namespace {

constexpr int NB = 8, DM = 512, HQ = 32, HKV = 8, SK = 8192, DH = 128, FF = 1792;
constexpr int NWG = 256, NTHR = 512;
constexpr int NT = NT_RING;
constexpr int TILES_PER_WAVE = 32;
static_assert(NT == 4, "ring = 8 pieces");
constexpr int FLY = FLY_PIECES;
static_assert(FLY >= 1 && FLY <= 6, "in-flight pieces");
constexpr float LN_EPS = 1e-5f;
constexpr float SCALE_LOG2E = 0.08838834764831845f * 1.4426950408889634f;

constexpr unsigned WS_EPOCH = 0;
constexpr unsigned WS_FLAG = 4096;
constexpr unsigned WS_Y = 8192;
constexpr unsigned WS_Q = WS_Y + NB * HKV * DM * 4;
constexpr unsigned REC_BYTES = 2304;
constexpr unsigned WS_REC = WS_Q + NB * 4096 * 8;
constexpr unsigned WS_STAMP = WS_REC + NWG * REC_BYTES;
constexpr unsigned WS_END = WS_STAMP + NWG * 16 * 8;

constexpr unsigned L_RING = 0;
constexpr unsigned L_STAGE = 4 * NT * 8192;
constexpr unsigned L_QBUF = L_STAGE + 16384;
constexpr unsigned L_PART = L_QBUF + 1024;
constexpr unsigned L_ML = L_PART + 8192;
constexpr unsigned L_STAT = L_ML + 128;
constexpr unsigned L_Y = L_STAT + 64;
constexpr unsigned L_T = L_Y + 2048;
constexpr unsigned L_MLP = L_T + 128;
constexpr unsigned L_CTL = L_MLP + 512;
constexpr unsigned L_END = L_CTL + 128;
static_assert(L_END <= 160 * 1024, "LDS budget");
enum { C_QRDY = 0, C_STAT = 1, C_MERGE = 2, C_YCNT = 3, C_STGFREE = 4, C_TLO = 6, C_THI = 7, C_TRDY = 8, C_NEAR = 9, C_MID = 10, C_FLAGOK = 11 };

constexpr unsigned SPIN_MAX = 1u << 18;

struct Params {
  const unsigned short* x; const unsigned short* k; const unsigned short* v; const unsigned short* wq; const unsigned short* wo;
  const unsigned short* w1; const unsigned short* b1; const unsigned short* w2; const unsigned short* b2;
  unsigned short* out; unsigned char* ws;
};

__device__ __forceinline__ v4i mkrsrc(const void* p, unsigned bytes) {
  const u64 a = (u64)p;
  v4i r; r.x = __builtin_amdgcn_readfirstlane((int)(unsigned)a); r.y = __builtin_amdgcn_readfirstlane((int)((unsigned)(a >> 32) & 0xffffu));
  r.z = (int)bytes; r.w = 0x00020000; return r;
}
__device__ __forceinline__ void dma4_nt(v4i rs, unsigned v0, unsigned v1, unsigned v2, unsigned v3, unsigned soff, unsigned lds) {
  asm volatile("s_mov_b32 m0, %6\n\ts_nop 0\n\t"
               "buffer_load_dwordx4 %0, %4, %5 offen nt lds\n\t"
               "buffer_load_dwordx4 %1, %4, %5 offen offset:1024 nt lds\n\t"
               "buffer_load_dwordx4 %2, %4, %5 offen offset:2048 nt lds\n\t"
               "buffer_load_dwordx4 %3, %4, %5 offen offset:3072 nt lds"
               :: "v"(v0), "v"(v1), "v"(v2), "v"(v3), "s"(rs), "s"(soff), "s"(lds) : "memory");
}
template <int NTP> __device__ __forceinline__ void dma1(v4i rs, unsigned voff, unsigned soff, unsigned lds) {
#define DMA1(POL) asm volatile("s_mov_b32 m0, %3\n\ts_nop 0\n\tbuffer_load_dwordx4 %0, %1, %2 offen " POL " lds" :: "v"(voff), "s"(rs), "s"(soff), "s"(lds) : "memory")
  if (NTP == 0) DMA1(""); else if (NTP == 1) DMA1("nt"); else if (NTP == 2) DMA1("sc1"); else if (NTP == 3) DMA1("sc0 sc1"); else if (NTP == 4) DMA1("sc1 nt"); else DMA1("sc0");
}
template <int N> __device__ __forceinline__ void wait_vm() { asm volatile("s_waitcnt vmcnt(%0)" :: "i"(N) : "memory"); }
__device__ __forceinline__ void wait_lgkm() { asm volatile("s_waitcnt lgkmcnt(0)" ::: "memory"); }
__device__ __forceinline__ void wg_barrier() { asm volatile("s_waitcnt lgkmcnt(0)\n\ts_barrier" ::: "memory"); }

template <int CTRL> __device__ __forceinline__ float dpp(float x) {
  return __builtin_bit_cast(float, __builtin_amdgcn_mov_dpp(__builtin_bit_cast(int, x), CTRL, 0xf, 0xf, true));
}
constexpr int DPP_XOR1 = 0xB1, DPP_XOR2 = 0x4E, DPP_HMIRROR = 0x141, DPP_ROR8 = 0x128, DPP_ROR4 = 0x124;
__device__ __forceinline__ float xrow_max(float x) {
  auto s = __builtin_amdgcn_permlane16_swap(__float_as_uint(x), __float_as_uint(x), false, false);
  x = fmaxf(__uint_as_float(s[0]), __uint_as_float(s[1]));
  auto t = __builtin_amdgcn_permlane32_swap(__float_as_uint(x), __float_as_uint(x), false, false);
  return fmaxf(__uint_as_float(t[0]), __uint_as_float(t[1]));
}
__device__ __forceinline__ float xrow_sum(float x) {
  auto s = __builtin_amdgcn_permlane16_swap(__float_as_uint(x), __float_as_uint(x), false, false);
  x = __uint_as_float(s[0]) + __uint_as_float(s[1]);
  auto t = __builtin_amdgcn_permlane32_swap(__float_as_uint(x), __float_as_uint(x), false, false);
  return __uint_as_float(t[0]) + __uint_as_float(t[1]);
}
__device__ __forceinline__ float row16_sum(float x) {
  x += dpp<DPP_XOR1>(x); x += dpp<DPP_XOR2>(x); x += dpp<DPP_HMIRROR>(x); x += dpp<DPP_ROR8>(x); return x;
}
__device__ __forceinline__ float wave_sum(float x) { return xrow_sum(row16_sum(x)); }
__device__ __forceinline__ unsigned pk_bf16(float lo, float hi) {
  bf16x2 r; r[0] = (__bf16)lo; r[1] = (__bf16)hi; return __builtin_bit_cast(unsigned, r);
}
__device__ __forceinline__ float bf_lo(unsigned u) { return __uint_as_float(u << 16); }
__device__ __forceinline__ float bf_hi(unsigned u) { return __uint_as_float(u & 0xffff0000u); }
__device__ __forceinline__ float bf1(unsigned short u) { return __uint_as_float((unsigned)u << 16); }
__device__ __forceinline__ f32x4 mfma32(v4u a, v4u b, f32x4 c) {
  return __builtin_amdgcn_mfma_f32_16x16x32_bf16(__builtin_bit_cast(bf16x8, a), __builtin_bit_cast(bf16x8, b), c, 0, 0, 0);
}
__device__ __forceinline__ f32x4 mfma16(v2u a, v2u b, f32x4 c) {
  return __builtin_amdgcn_mfma_f32_16x16x16bf16_1k(__builtin_bit_cast(s16x4, a), __builtin_bit_cast(s16x4, b), c, 0, 0, 0);
}
__device__ __forceinline__ v2u tr_read(LAS unsigned char* p) {
  return __builtin_bit_cast(v2u, __builtin_amdgcn_ds_read_tr16_b64_v4i16((LAS s16x4*)p));
}
__device__ __forceinline__ v4u ld_sc1(__amdgpu_buffer_rsrc_t rs, unsigned off) { return __builtin_amdgcn_raw_buffer_load_b128(rs, (int)off, 0, 16); }
__device__ __forceinline__ void st_sc1(__amdgpu_buffer_rsrc_t rs, unsigned off, v4u v) { __builtin_amdgcn_raw_buffer_store_b128(v, rs, (int)off, 0, 16); }
__device__ __forceinline__ unsigned lds_ld(LAS unsigned char* L, int idx) { return *(volatile LAS unsigned*)(L + L_CTL + 4 * idx); }
__device__ __forceinline__ void lds_st(LAS unsigned char* L, int idx, unsigned v) { *(volatile LAS unsigned*)(L + L_CTL + 4 * idx) = v; }
__device__ __forceinline__ void lds_wait(LAS unsigned char* L, int idx, unsigned want) {
  for (unsigned s = 0; s < SPIN_MAX; ++s) { if (lds_ld(L, idx) >= want) break; __builtin_amdgcn_s_sleep(1); }
  asm volatile("" ::: "memory");
}
__device__ __forceinline__ unsigned tagf(float f) { return __float_as_uint(f) | 1u; }
#define STAMP(i) do { if ((threadIdx.x & 63) == 0) *(volatile LAS u64*)(L + L_END + 8 * (i)) = __builtin_amdgcn_s_memrealtime(); } while (0)

struct AttnState { float m, l; f32x4 o[8]; };

struct StreamCtx {
  LAS unsigned char* L; unsigned ring; v4i rk, rv; unsigned vk[4], vv[2], ka[4], va[8]; unsigned gbase; int sp, wave;
  __device__ __forceinline__ unsigned toff(int j) const {
    return gbase + (unsigned)(16 * j + 4 * ((sp + (j >> 2)) & 3) + ((wave + j) & 3)) * 4096u;
  }
  template <int KIND> __device__ __forceinline__ void issue(int j, int slot) const {
    if (KIND == 0) dma4_nt(rk, vk[0], vk[1], vk[2], vk[3], toff(j), ring + slot * 4096);
    else dma4_nt(rv, vv[0], vv[1], vv[0], vv[1], toff(j), ring + slot * 4096);
  }
};
constexpr int NPIECE = 2 * TILES_PER_WAVE;
constexpr int AHEAD = 16;
constexpr int wait_count(int n) {
  int last = n + FLY - 1;
  if (n >= 8 && last < AHEAD - 1) last = AHEAD - 1;
  if (last > NPIECE - 1) last = NPIECE - 1;
  return 4 * (last - n);
}
constexpr bool do_issue(int n) { return (n < 8 || n + FLY >= AHEAD) && n + FLY < NPIECE; }

__device__ __forceinline__ void softmax_pv(AttnState& st, const f32x4 sc, const v2u (&vf)[8], bool lds_pending) {
  float s0 = sc[0] * SCALE_LOG2E, s1 = sc[1] * SCALE_LOG2E, s2 = sc[2] * SCALE_LOG2E, s3 = sc[3] * SCALE_LOG2E;
  float tm = xrow_max(fmaxf(fmaxf(s0, s1), fmaxf(s2, s3)));
  if (__builtin_amdgcn_ballot_w64(tm > st.m) != 0ull) {
    const float mn = fmaxf(st.m, tm);
    const float al = __builtin_amdgcn_exp2f(st.m - mn);
    st.m = mn; st.l *= al;
    const float a0 = dpp<0x00>(al), a1 = dpp<0x55>(al), a2 = dpp<0xAA>(al), a3 = dpp<0xFF>(al);
#pragma unroll
    for (int c = 0; c < 8; ++c) { st.o[c][0] *= a0; st.o[c][1] *= a1; st.o[c][2] *= a2; st.o[c][3] *= a3; }
  }
  const float p0 = __builtin_amdgcn_exp2f(s0 - st.m), p1 = __builtin_amdgcn_exp2f(s1 - st.m);
  const float p2 = __builtin_amdgcn_exp2f(s2 - st.m), p3 = __builtin_amdgcn_exp2f(s3 - st.m);
  st.l += (p0 + p1) + (p2 + p3);
  v2u pf; pf.x = pk_bf16(p0, p1); pf.y = pk_bf16(p2, p3);
  if (lds_pending) wait_lgkm();
#pragma unroll
  for (int c = 0; c < 8; ++c) st.o[c] = mfma16(pf, vf[c], st.o[c]);
}
template <int TC>
__device__ __forceinline__ void attn_tile(AttnState& st, const StreamCtx& cx, const v4u (&qf)[4], int t) {
  constexpr int n0 = 2 * TC, n1 = 2 * TC + 1;
  LAS unsigned char* kb = cx.L + cx.ring + (n0 & 7) * 4096;
  wait_vm<wait_count(n0)>();
  if constexpr (do_issue(n0)) cx.issue<(n0 + FLY) & 1>(t + (FLY >> 1), (n0 + FLY) & 7);
  v4u kf[4];
#pragma unroll
  for (int s = 0; s < 4; ++s) kf[s] = *(LAS v4u*)(kb + cx.ka[s]);
  f32x4 sc = {0.f, 0.f, 0.f, 0.f};
#pragma unroll
  for (int s = 0; s < 4; ++s) sc = mfma32(kf[s], qf[s], sc);
  wait_vm<wait_count(n1)>();
  if constexpr (do_issue(n1)) cx.issue<(n1 + FLY) & 1>(t + ((FLY + 1) >> 1), (n1 + FLY) & 7);
  v2u vf[8];
#pragma unroll
  for (int c = 0; c < 8; ++c) vf[c] = tr_read(kb + 4096 + cx.va[c]);
  softmax_pv(st, sc, vf, true);
}
struct ParkedTile { v4u kf[4]; v2u vf[8]; };
template <int TC>
__device__ __forceinline__ void park_tile(ParkedTile& pt, const StreamCtx& cx) {
  constexpr int n0 = 2 * TC, n1 = 2 * TC + 1;
  LAS unsigned char* kb = cx.L + cx.ring + (n0 & 7) * 4096;
  wait_vm<wait_count(n0)>();
  cx.issue<(n0 + FLY) & 1>(TC + (FLY >> 1), (n0 + FLY) & 7);
#pragma unroll
  for (int s = 0; s < 4; ++s) pt.kf[s] = *(LAS v4u*)(kb + cx.ka[s]);
  wait_vm<wait_count(n1)>();
  cx.issue<(n1 + FLY) & 1>(TC + ((FLY + 1) >> 1), (n1 + FLY) & 7);
#pragma unroll
  for (int c = 0; c < 8; ++c) pt.vf[c] = tr_read(kb + 4096 + cx.va[c]);
  wait_lgkm();
}
__device__ __forceinline__ void parked_compute(AttnState& st, const ParkedTile& pt, const v4u (&qf)[4]) {
  f32x4 sc = {0.f, 0.f, 0.f, 0.f};
#pragma unroll
  for (int s = 0; s < 4; ++s) sc = mfma32(pt.kf[s], qf[s], sc);
  softmax_pv(st, sc, pt.vf, false);
}
template <int N> __device__ __forceinline__ void first_issue(const StreamCtx& cx) {
  cx.issue<N & 1>(N >> 1, N);
  if constexpr (N + 1 < FLY) first_issue<N + 1>(cx);
}
template <int N> __device__ __forceinline__ void ahead_issue(const StreamCtx& cx) {
  if constexpr (N < AHEAD) {
    wait_vm<4 * (FLY - 1)>();
    cx.issue<N & 1>(N >> 1, N & 7);
    ahead_issue<N + 1>(cx);
  }
}

__global__ void __launch_bounds__(NTHR, 2) fused_decode_kernel(Params p) {
  extern __shared__ __attribute__((aligned(16))) unsigned char lds_raw[];
  LAS unsigned char* L = (LAS unsigned char*)lds_raw;
  const int tid = threadIdx.x, lane = tid & 63, wave = __builtin_amdgcn_readfirstlane(tid >> 6);
  const int c = blockIdx.x;
  const int b = c >> 5, kvh = c & 7, sp = (c >> 3) & 3, grp = b * 8 + kvh;
  const int rid = grp * 4 + sp;
  const int cq = (c & 7) * 32 + (c >> 3);
  const int e16 = c & 31;
  const int g = lane >> 4, c16 = lane & 15;
  const __amdgpu_buffer_rsrc_t rws = __builtin_amdgcn_make_buffer_rsrc(p.ws, 0, WS_END, 0x00020000);

  float k3[7], k2[7], k1[7], k0[7], wa[7], wb[7]; unsigned short b2r;
  auto load_params = [&]() {
    constexpr float K0 = -2.f * 0.7978845608028654f * 1.4426950408889634f, K1 = K0 * 0.044715f;
    unsigned short w1r[7], b1r[7], w2r[7];
    const int f0 = tid & 255;
#pragma unroll
    for (int i = 0; i < 7; ++i) { w1r[i] = p.w1[f0 + 256 * i]; b1r[i] = p.b1[f0 + 256 * i]; w2r[i] = p.w2[f0 + 256 * i]; }
    b2r = p.b2[0];
#pragma unroll
    for (int i = 0; i < 7; ++i) {
      const float a = bf1(w1r[i]), bb = bf1(b1r[i]), w2 = bf1(w2r[i]);
      k3[i] = K1 * a * a * a; k2[i] = 3.f * K1 * a * a * bb; k1[i] = a * fmaf(3.f * K1 * bb, bb, K0); k0[i] = bb * fmaf(K1 * bb, bb, K0);
      wa[i] = w2 * a; wb[i] = w2 * bb;
    }
  };

  if (wave < 4) {
    if (wave == 0) STAMP(0);
    StreamCtx cx;
    cx.L = L; cx.rk = mkrsrc(p.k, 1u << 27); cx.rv = mkrsrc(p.v, 1u << 27); cx.sp = sp; cx.wave = wave;
#pragma unroll
    for (int i = 0; i < 4; ++i) cx.vk[i] = X_NOSWZ ? lane * 16 : g * 256 + 16 * (c16 ^ (4 * i + g));
#pragma unroll
    for (int i = 0; i < 2; ++i) cx.vv[i] = X_NOSWZ ? lane * 16 : g * 256 + 16 * (c16 ^ (((4 * i + g) & 7) << 1));
#pragma unroll
    for (int s = 0; s < 4; ++s) cx.ka[s] = 256 * c16 + 16 * ((4 * s + g) ^ c16);
    {
      const int q4 = c16 >> 2, p4 = lane & 3, row = 4 * g + q4;
#pragma unroll
      for (int cc = 0; cc < 8; ++cc) cx.va[cc] = 256 * row + 16 * ((2 * cc + (p4 >> 1)) ^ ((row & 7) << 1)) + 8 * (p4 & 1);
    }
    cx.ring = L_RING + wave * (NT * 8192);
    cx.gbase = (unsigned)grp * (2048u * 1024u);
    first_issue<0>(cx);
    if (wave == 0) STAMP(10);
    wg_barrier();
    ParkedTile pk0, pk1, pk2, pk3;
    park_tile<0>(pk0, cx); park_tile<1>(pk1, cx); park_tile<2>(pk2, cx); park_tile<3>(pk3, cx);
    if (wave == 0) STAMP(11);
    ahead_issue<8 + FLY>(cx);
    if (wave == 0) STAMP(12);
    lds_wait(L, C_QRDY, 1u);
    v4u qf[4];
#pragma unroll
    for (int s = 0; s < 4; ++s) qf[s] = *(LAS v4u*)(L + L_QBUF + (lane & 3) * 256 + s * 64 + g * 16);
    AttnState st; st.m = -INFINITY; st.l = 0.f;
#pragma unroll
    for (int cc = 0; cc < 8; ++cc) st.o[cc] = f32x4{0.f, 0.f, 0.f, 0.f};
    attn_tile<4>(st, cx, qf, 4); attn_tile<5>(st, cx, qf, 5); attn_tile<6>(st, cx, qf, 6); attn_tile<7>(st, cx, qf, 7);
    parked_compute(st, pk0, qf); attn_tile<8>(st, cx, qf, 8);
    parked_compute(st, pk1, qf); attn_tile<9>(st, cx, qf, 9);
    parked_compute(st, pk2, qf); attn_tile<10>(st, cx, qf, 10);
    parked_compute(st, pk3, qf); attn_tile<11>(st, cx, qf, 11);
    if (wave == 0) STAMP(13);
    if (wave == 0) lds_st(L, C_MID, 1u);
    for (int j = 12; j < TILES_PER_WAVE - 4; j += 4) {
      if (wave == 0 && j == 20) STAMP(14);
      attn_tile<12>(st, cx, qf, j); attn_tile<13>(st, cx, qf, j + 1); attn_tile<14>(st, cx, qf, j + 2); attn_tile<15>(st, cx, qf, j + 3);
    }
    if (wave == 0) STAMP(15);
    attn_tile<28>(st, cx, qf, 28); attn_tile<29>(st, cx, qf, 29);
    if (wave == 0) lds_st(L, C_NEAR, 1u);
    attn_tile<30>(st, cx, qf, 30); attn_tile<31>(st, cx, qf, 31);
    if (wave == 0) STAMP(2);
    const float lsum = xrow_sum(st.l);
#pragma unroll
    for (int cc = 0; cc < 8; ++cc) {
      const float v = g == 0 ? st.o[cc][0] : g == 1 ? st.o[cc][1] : g == 2 ? st.o[cc][2] : st.o[cc][3];
      *(LAS float*)(L + L_PART + wave * 2048 + g * 512 + (16 * cc + c16) * 4) = v;
    }
    if (lane < 4) *(LAS v2u*)(L + L_ML + wave * 32 + lane * 8) = v2u{__float_as_uint(st.m), __float_as_uint(lsum)};
    wait_lgkm();
    unsigned old = 0;
    if (lane == 0) old = __hip_atomic_fetch_add((LAS unsigned*)(L + L_CTL + 4 * C_MERGE), 1u, __ATOMIC_RELAXED, __HIP_MEMORY_SCOPE_WORKGROUP);
    old = __builtin_amdgcn_readfirstlane(old);
    asm volatile("" ::: "memory");
    if (old == 3u) {
      float mw[4], lw[4];
#pragma unroll
      for (int w = 0; w < 4; ++w) { const v2u ml = *(LAS v2u*)(L + L_ML + w * 32 + g * 8); mw[w] = __uint_as_float(ml.x); lw[w] = __uint_as_float(ml.y); }
      const float M = fmaxf(fmaxf(mw[0], mw[1]), fmaxf(mw[2], mw[3]));
      float Ls = 0.f; f32x4 a0 = {0.f, 0.f, 0.f, 0.f}, a1 = {0.f, 0.f, 0.f, 0.f};
#pragma unroll
      for (int w = 0; w < 4; ++w) {
        const float f = __builtin_amdgcn_exp2f(mw[w] - M);
        const f32x4 x0 = *(LAS f32x4*)(L + L_PART + w * 2048 + g * 512 + c16 * 16), x1 = *(LAS f32x4*)(L + L_PART + w * 2048 + g * 512 + 256 + c16 * 16);
        Ls = fmaf(f, lw[w], Ls); a0 += f * x0; a1 += f * x1;
      }
      const unsigned rec = WS_REC + (unsigned)rid * REC_BYTES;
      st_sc1(rws, rec + g * 512 + c16 * 16, v4u{tagf(a0[0]), tagf(a0[1]), tagf(a0[2]), tagf(a0[3])});
      st_sc1(rws, rec + g * 512 + 256 + c16 * 16, v4u{tagf(a1[0]), tagf(a1[1]), tagf(a1[2]), tagf(a1[3])});
      if (c16 == 0) __builtin_amdgcn_raw_buffer_store_b64(v2u{tagf(M), tagf(Ls)}, rws, (int)(rec + 2048 + g * 8), 0, 16);
      STAMP(3);
    }
    load_params();
  } else {
    const int hw = wave - 4;
    if (hw == 0) {
      const v4i rq = mkrsrc(p.wq, DM * HQ * DH * 2);
      const unsigned vo = (lane >> 1) * 8192 + (lane & 1) * 16;
#pragma unroll
      for (int s = 0; s < 16; ++s) if (!X_NOWQ) {
        if (X_KSPLIT) dma1<WQ_NT>(rq, (lane >> 3) * 8192 + (lane & 7) * 16, (unsigned)(cq >> 2) * 128u + (unsigned)((cq & 3) * 128 + s * 8) * 8192u, L_STAGE + s * 1024);
        else dma1<WQ_NT>(rq, vo, (unsigned)cq * 32u + (unsigned)s * (32u * 8192u), L_STAGE + s * 1024);
      }
      v4u xf[16];
      const GAS v4u* xp = (const GAS v4u*)(p.x + (c16 & 7) * DM + g * 8);
#pragma unroll
      for (int s = 0; s < 16; ++s) xf[s] = xp[s * 4];
      const u64 ep = __hip_atomic_load((GAS u64*)(p.ws + WS_EPOCH), __ATOMIC_RELAXED, __HIP_MEMORY_SCOPE_AGENT);
      const u64 T = ep + 0x9E3779B97F4A7C15ull;
      wg_barrier();
      if (lane == 0) { lds_st(L, C_TLO, (unsigned)T); lds_st(L, C_THI, (unsigned)(T >> 32)); wait_lgkm(); lds_st(L, C_TRDY, 1u); }
      if (c16 >= 8) {
#pragma unroll
        for (int s = 0; s < 16; ++s) xf[s] = v4u{0x3f803f80u, 0x3f803f80u, 0x3f803f80u, 0x3f803f80u};
      }
      wait_vm<0>();
      STAMP(1);
      f32x4 d = {0.f, 0.f, 0.f, 0.f};
      const unsigned ta = (8 * g + (c16 >> 2)) * 32 + (lane & 3) * 8;
#pragma unroll
      for (int s = 0; s < 16; ++s) {
        const v2u lo = tr_read(L + L_STAGE + s * 1024 + ta), hi = tr_read(L + L_STAGE + s * 1024 + ta + 128);
        d = mfma32(v4u{lo.x, lo.y, hi.x, hi.y}, xf[s], d);
      }
      lds_wait(L, C_STAT, 1u);
      const float mean = *(LAS float*)(L + L_STAT + (c16 & 7) * 8), rstd = *(LAS float*)(L + L_STAT + (c16 & 7) * 8 + 4);
      unsigned gl[4];
#pragma unroll
      for (int r = 0; r < 4; ++r) {
        const float cs = dpp<DPP_ROR8>(d[r]);
        const float q = rstd * (d[r] - mean * cs);
        gl[r] = (pk_bf16(q, q) & 0xffffu) | ((unsigned)T << 16);
      }
      const unsigned thi = (unsigned)(T >> 16);
      if (c16 < 8) {
        const unsigned off = WS_Q + ((unsigned)c16 * 4096u + (unsigned)cq * 16u + g * 4u) * 8u;
        st_sc1(rws, off, v4u{gl[0], thi, gl[1], thi});
        st_sc1(rws, off + 16, v4u{gl[2], thi, gl[3], thi});
      }
      STAMP(4);
      const unsigned qoff = WS_Q + ((unsigned)b * 4096u + (unsigned)kvh * 512u) * 8u + lane * 16u;
      const unsigned tlo = (unsigned)T << 16;
      v4u a0, a1, a2, a3; unsigned bad = 1u;
      for (unsigned s = 0; s < SPIN_MAX; ++s) {
        asm volatile("" ::: "memory");
        if (bad != 0u) {
          a0 = ld_sc1(rws, qoff); a1 = ld_sc1(rws, qoff + 1024); a2 = ld_sc1(rws, qoff + 2048); a3 = ld_sc1(rws, qoff + 3072);
          bad = (((a0.x ^ tlo) | (a0.z ^ tlo) | (a1.x ^ tlo) | (a1.z ^ tlo) | (a2.x ^ tlo) | (a2.z ^ tlo) | (a3.x ^ tlo) | (a3.z ^ tlo)) >> 16) |
                (a0.y ^ thi) | (a0.w ^ thi) | (a1.y ^ thi) | (a1.w ^ thi) | (a2.y ^ thi) | (a2.w ^ thi) | (a3.y ^ thi) | (a3.w ^ thi);
        }
        if (X_NOGATHER || __builtin_amdgcn_ballot_w64(bad != 0u) == 0ull) break;
        __builtin_amdgcn_s_sleep(X_QSLEEP);
      }
      *(LAS unsigned*)(L + L_QBUF + lane * 4) = (a0.x & 0xffffu) | (a0.z << 16);
      *(LAS unsigned*)(L + L_QBUF + 256 + lane * 4) = (a1.x & 0xffffu) | (a1.z << 16);
      *(LAS unsigned*)(L + L_QBUF + 512 + lane * 4) = (a2.x & 0xffffu) | (a2.z << 16);
      *(LAS unsigned*)(L + L_QBUF + 768 + lane * 4) = (a3.x & 0xffffu) | (a3.z << 16);
      wait_lgkm();
      lds_st(L, C_QRDY, 1u);
      STAMP(5);
    } else if (hw == 1) {
      v4u xv[8];
      const GAS v4u* xp = (const GAS v4u*)(p.x + (lane >> 3) * DM) + (lane & 7);
#pragma unroll
      for (int i = 0; i < 8; ++i) xv[i] = xp[i * 8];
      wg_barrier();
      float s = 0.f;
#pragma unroll
      for (int i = 0; i < 8; ++i)
#pragma unroll
        for (int e = 0; e < 4; ++e) s += bf_lo(xv[i][e]) + bf_hi(xv[i][e]);
      s += dpp<DPP_XOR1>(s); s += dpp<DPP_XOR2>(s); s += dpp<DPP_HMIRROR>(s);
      const float mean = s * (1.f / DM);
      float vs = 0.f;
#pragma unroll
      for (int i = 0; i < 8; ++i)
#pragma unroll
        for (int e = 0; e < 4; ++e) { const float d0 = bf_lo(xv[i][e]) - mean, d1 = bf_hi(xv[i][e]) - mean; vs = fmaf(d0, d0, vs); vs = fmaf(d1, d1, vs); }
      vs += dpp<DPP_XOR1>(vs); vs += dpp<DPP_XOR2>(vs); vs += dpp<DPP_HMIRROR>(vs);
      const float rstd = 1.f / sqrtf(vs * (1.f / DM) + LN_EPS);
      if ((lane & 7) == 0) { *(LAS float*)(L + L_STAT + (lane >> 3) * 8) = mean; *(LAS float*)(L + L_STAT + (lane >> 3) * 8 + 4) = rstd; }
      wait_lgkm();
      lds_st(L, C_STAT, 1u);
    } else if (hw == 2) {
      wg_barrier();
    } else {
      if (lane < 32) lds_st(L, lane, 0u);
      wg_barrier();
    }
    lds_wait(L, C_QRDY, 1u);
    const u64 T = (u64)lds_ld(L, C_TLO) | ((u64)lds_ld(L, C_THI) << 32);
    if (X_LATEMISC) lds_wait(L, C_MID, 1u);
    const unsigned yslot = WS_Y + (((unsigned)b * 8u + (unsigned)kvh) * 512u + (unsigned)sp * 128u) * 4u;
    if (hw == 2) {
      const v4u z = {0u, 0u, 0u, 0u};
      const unsigned rec = WS_REC + (unsigned)rid * REC_BYTES;
      st_sc1(rws, rec + lane * 16, z); st_sc1(rws, rec + 1024 + lane * 16, z);
      if (lane < 2) st_sc1(rws, rec + 2048 + lane * 16, z);
      if (lane < 32) st_sc1(rws, yslot + lane * 16, z);
      wait_vm<0>();
      if (lane == 0) __hip_atomic_store((GAS u64*)(p.ws + WS_FLAG) + c, T, __ATOMIC_RELAXED, __HIP_MEMORY_SCOPE_AGENT);
    }
    v2u wf[8][8];
    {
      const v4i ro = mkrsrc(p.wo, HQ * DH * DM * 2);
      const unsigned stg = L_STAGE + hw * 4096;
      const int q4 = c16 >> 2, p4 = lane & 3, row = 4 * g + q4;
      const unsigned vo0 = g * 1024 + 16 * (c16 ^ ((g & 7) << 1)), vo1 = g * 1024 + 16 * (c16 ^ (((4 + g) & 7) << 1));
      const unsigned ra = 256 * row + 16 * ((p4 >> 1) ^ ((row & 7) << 1)) + 8 * (p4 & 1);
#pragma unroll
      for (int ch = 0; ch < 8; ++ch) {
        const unsigned so = (unsigned)(kvh * 512 + hw * 128 + ch * 16) * 1024u + (unsigned)sp * 256u;
#pragma unroll
        for (int i = 0; i < 4; ++i) if (!X_NOWO) dma1<X_WONT>(ro, (i & 1) ? vo1 : vo0, so + i * 4096u, stg + i * 1024);
        wait_vm<0>();
#pragma unroll
        for (int cc = 0; cc < 8; ++cc) wf[ch][cc] = tr_read(L + stg + (ra ^ (unsigned)(cc << 5)));
        wait_lgkm();
      }
    }
    load_params();
    v4u xr0, xr1;
    { const v2u t0 = *(const GAS v2u*)(p.x + b * DM + lane * 4), t1 = *(const GAS v2u*)(p.x + b * DM + 256 + lane * 4);
      xr0 = v4u{t0.x << 16, t0.x & 0xffff0000u, t0.y << 16, t0.y & 0xffff0000u}; xr1 = v4u{t1.x << 16, t1.x & 0xffff0000u, t1.y << 16, t1.y & 0xffff0000u}; }
    if (hw == 3) {
      lds_wait(L, C_MID, 1u);
      const GAS u64* fp = (const GAS u64*)(p.ws + WS_FLAG) + b * 32 + (lane & 31);
      for (unsigned s = 0; s < SPIN_MAX; ++s) {
        const u64 f = __hip_atomic_load(fp, __ATOMIC_RELAXED, __HIP_MEMORY_SCOPE_AGENT);
        if (__builtin_amdgcn_ballot_w64(f != T) == 0ull) break;
        __builtin_amdgcn_s_sleep(64);
      }
      asm volatile("" ::: "memory");
      lds_st(L, C_FLAGOK, 1u);
    }
    lds_wait(L, C_FLAGOK, 1u);
    if (hw == 0) STAMP(6);
    lds_wait(L, X_H1 ? C_MERGE : C_NEAR, X_H1 ? (unsigned)X_H1 : 1u);
    {
      const int sg = lane & 3, jq = (lane >> 2) & 3;
      const unsigned rec = WS_REC + (unsigned)(grp * 4 + sg) * REC_BYTES;
      const unsigned doff = rec + hw * 512 + (32 * jq + 4 * g) * 4;
      v4u d0, d1; v2u hd; unsigned ok = 0u;
      for (unsigned s = 0; s < SPIN_MAX; ++s) {
        asm volatile("" ::: "memory");
        if ((ok & 1u) == 0u) {
          hd = __builtin_amdgcn_raw_buffer_load_b64(rws, (int)(rec + 2048 + hw * 8), 0, 16);
          d0 = ld_sc1(rws, doff); d1 = ld_sc1(rws, doff + 64);
          ok = hd.x & hd.y & d0.x & d0.y & d0.z & d0.w & d1.x & d1.y & d1.z & d1.w;
        }
        if (__builtin_amdgcn_ballot_w64((ok & 1u) == 0u) == 0ull) break;
        __builtin_amdgcn_s_sleep(1);
      }
      const float mh = __uint_as_float(hd.x), lh = __uint_as_float(hd.y);
      float mx = fmaxf(mh, dpp<DPP_XOR1>(mh)); mx = fmaxf(mx, dpp<DPP_XOR2>(mx));
      const float e = __builtin_amdgcn_exp2f(mh - mx);
      float lt = e * lh; lt += dpp<DPP_XOR1>(lt); lt += dpp<DPP_XOR2>(lt);
      const float w = e / lt;
      const v4u af = {pk_bf16(__uint_as_float(d0.x) * w, __uint_as_float(d0.y) * w), pk_bf16(__uint_as_float(d0.z) * w, __uint_as_float(d0.w) * w),
                      pk_bf16(__uint_as_float(d1.x) * w, __uint_as_float(d1.y) * w), pk_bf16(__uint_as_float(d1.z) * w, __uint_as_float(d1.w) * w)};
      f32x4 acc[8];
#pragma unroll
      for (int cc = 0; cc < 8; ++cc) acc[cc] = f32x4{0.f, 0.f, 0.f, 0.f};
#pragma unroll
      for (int j = 0; j < 4; ++j) {
        const v4u aj = jq == j ? af : v4u{0u, 0u, 0u, 0u};
#pragma unroll
        for (int cc = 0; cc < 8; ++cc) acc[cc] = mfma32(aj, v4u{wf[2 * j][cc].x, wf[2 * j][cc].y, wf[2 * j + 1][cc].x, wf[2 * j + 1][cc].y}, acc[cc]);
      }
      float sv[8];
#pragma unroll
      for (int cc = 0; cc < 8; ++cc) sv[cc] = (acc[cc][0] + acc[cc][1]) + (acc[cc][2] + acc[cc][3]);
      float r2[2];
#pragma unroll
      for (int hh = 0; hh < 2; ++hh) {
        auto s01 = __builtin_amdgcn_permlane16_swap(__float_as_uint(sv[4 * hh]), __float_as_uint(sv[4 * hh + 1]), false, false);
        auto s23 = __builtin_amdgcn_permlane16_swap(__float_as_uint(sv[4 * hh + 2]), __float_as_uint(sv[4 * hh + 3]), false, false);
        const float x = __uint_as_float(s01[0]) + __uint_as_float(s01[1]), y = __uint_as_float(s23[0]) + __uint_as_float(s23[1]);
        auto t = __builtin_amdgcn_permlane32_swap(__float_as_uint(x), __float_as_uint(y), false, false);
        r2[hh] = __uint_as_float(t[0]) + __uint_as_float(t[1]);
      }
      *(LAS float*)(L + L_Y + hw * 512 + lane * 4) = r2[0];
      *(LAS float*)(L + L_Y + hw * 512 + 256 + lane * 4) = r2[1];
    }
    wait_lgkm();
    unsigned old = 0;
    if (lane == 0) old = __hip_atomic_fetch_add((LAS unsigned*)(L + L_CTL + 4 * C_YCNT), 1u, __ATOMIC_RELAXED, __HIP_MEMORY_SCOPE_WORKGROUP);
    old = __builtin_amdgcn_readfirstlane(old);
    asm volatile("" ::: "memory");
    if (old == 3u) {
      STAMP(7);
      if (lane < 32) {
        const f32x4 y0 = *(LAS f32x4*)(L + L_Y + lane * 16), y1 = *(LAS f32x4*)(L + L_Y + 512 + lane * 16);
        const f32x4 y2 = *(LAS f32x4*)(L + L_Y + 1024 + lane * 16), y3 = *(LAS f32x4*)(L + L_Y + 1536 + lane * 16);
        const f32x4 y = (y0 + y1) + (y2 + y3);
        st_sc1(rws, yslot + lane * 16, v4u{tagf(y[0]), tagf(y[1]), tagf(y[2]), tagf(y[3])});
      }
      const unsigned yoff = WS_Y + (unsigned)b * 16384u + lane * 16;
      {
        const unsigned ioff = WS_Y + (unsigned)b * 16384u + (lane & 31) * 512u + 508u;
        unsigned iv = 0u;
        for (unsigned s = 0; s < SPIN_MAX; ++s) {
          asm volatile("" ::: "memory");
          if ((iv & 1u) == 0u) iv = __builtin_amdgcn_raw_buffer_load_b32(rws, (int)ioff, 0, 16);
          if (__builtin_popcount((unsigned)__builtin_amdgcn_ballot_w64((iv & 1u) == 0u)) <= X_H2) break;
          __builtin_amdgcn_s_sleep(4);
        }
      }
      v4u yv[16]; unsigned pend = 0xffffu;
      for (unsigned s = 0; s < SPIN_MAX; ++s) {
        asm volatile("" ::: "memory");
#pragma unroll
        for (int i = 0; i < 16; ++i) if ((pend >> i) & 1u) yv[i] = ld_sc1(rws, yoff + i * 1024);
#pragma unroll
        for (int i = 0; i < 16; ++i) if ((pend >> i) & 1u) {
          const unsigned ok = yv[i].x & yv[i].y & yv[i].z & yv[i].w;
          if (__builtin_amdgcn_ballot_w64((ok & 1u) == 0u) == 0ull) pend &= ~(1u << i);
        }
        if (pend == 0u) break;
      }
      if (c == 0 && lane == 0) __hip_atomic_store((GAS u64*)(p.ws + WS_EPOCH), T, __ATOMIC_RELAXED, __HIP_MEMORY_SCOPE_AGENT);
      float hv[8];
#pragma unroll
      for (int e = 0; e < 4; ++e) {
        float s0 = 0.f, s1 = 0.f;
#pragma unroll
        for (int kk = 0; kk < 8; ++kk) { s0 += __uint_as_float(yv[2 * kk][e]); s1 += __uint_as_float(yv[2 * kk + 1][e]); }
        hv[e] = __uint_as_float(xr0[e]) + s0; hv[4 + e] = __uint_as_float(xr1[e]) + s1;
      }
      float z[8];
      {
        float s = 0.f;
#pragma unroll
        for (int e = 0; e < 8; ++e) s += hv[e];
        const float m1 = wave_sum(s) * (1.f / DM);
        float vs = 0.f;
#pragma unroll
        for (int e = 0; e < 8; ++e) { z[e] = hv[e] - m1; vs = fmaf(z[e], z[e], vs); }
        const float r1 = 1.f / sqrtf(wave_sum(vs) * (1.f / DM) + LN_EPS);
        s = 0.f;
#pragma unroll
        for (int e = 0; e < 8; ++e) { z[e] *= r1; s += z[e]; }
        const float m2 = wave_sum(s) * (1.f / DM);
        vs = 0.f;
#pragma unroll
        for (int e = 0; e < 8; ++e) { z[e] -= m2; vs = fmaf(z[e], z[e], vs); }
        const float r2 = 1.f / sqrtf(wave_sum(vs) * (1.f / DM) + LN_EPS);
#pragma unroll
        for (int e = 0; e < 8; ++e) z[e] *= r2;
      }
      const int l0 = (4 * e16) & 63;
      if (lane >= l0 && lane < l0 + 4) {
        const bool hi = e16 >= 16;
        const f32x4 tz = {hi ? z[4] : z[0], hi ? z[5] : z[1], hi ? z[6] : z[2], hi ? z[7] : z[3]};
        const f32x4 th = {hi ? hv[4] : hv[0], hi ? hv[5] : hv[1], hi ? hv[6] : hv[2], hi ? hv[7] : hv[3]};
        *(LAS f32x4*)(L + L_T + (lane - l0) * 16) = tz;
        *(LAS f32x4*)(L + L_T + 64 + (lane - l0) * 16) = th;
      }
      STAMP(8);
    }
  }
  wg_barrier();
  {
    const int eh = wave >> 2;
    const f32x4 t0 = *(LAS f32x4*)(L + L_T + eh * 32), t1 = *(LAS f32x4*)(L + L_T + eh * 32 + 16);
    const f32x2 tp[4] = {f32x2{t0[0], t0[1]}, f32x2{t0[2], t0[3]}, f32x2{t1[0], t1[1]}, f32x2{t1[2], t1[3]}};
    f32x2 acc[4];
#pragma unroll
    for (int e = 0; e < 4; ++e) acc[e] = f32x2{0.f, 0.f};
#pragma unroll
    for (int i = 0; i < 7; ++i) {
#pragma unroll
      for (int e = 0; e < 4; ++e) {
        const f32x2 t = tp[e];
        f32x2 ar = __builtin_elementwise_fma(f32x2{k3[i], k3[i]}, t, f32x2{k2[i], k2[i]});
        ar = __builtin_elementwise_fma(ar, t, f32x2{k1[i], k1[i]});
        ar = __builtin_elementwise_fma(ar, t, f32x2{k0[i], k0[i]});
        const f32x2 wu = __builtin_elementwise_fma(f32x2{wa[i], wa[i]}, t, f32x2{wb[i], wb[i]});
        f32x2 d = {__builtin_amdgcn_exp2f(ar.x), __builtin_amdgcn_exp2f(ar.y)};
        d += f32x2{1.f, 1.f};
        const f32x2 r = {__builtin_amdgcn_rcpf(d.x), __builtin_amdgcn_rcpf(d.y)};
        acc[e] = __builtin_elementwise_fma(wu, r, acc[e]);
      }
    }
    float r2[2];
#pragma unroll
    for (int hh = 0; hh < 2; ++hh) {
      auto s01 = __builtin_amdgcn_permlane16_swap(__float_as_uint(acc[2 * hh].x), __float_as_uint(acc[2 * hh].y), false, false);
      auto s23 = __builtin_amdgcn_permlane16_swap(__float_as_uint(acc[2 * hh + 1].x), __float_as_uint(acc[2 * hh + 1].y), false, false);
      const float x = __uint_as_float(s01[0]) + __uint_as_float(s01[1]), y = __uint_as_float(s23[0]) + __uint_as_float(s23[1]);
      auto t = __builtin_amdgcn_permlane32_swap(__float_as_uint(x), __float_as_uint(y), false, false);
      r2[hh] = row16_sum(__uint_as_float(t[0]) + __uint_as_float(t[1]));
    }
    if (c16 == 0) { *(LAS float*)(L + L_MLP + wave * 32 + g * 4) = r2[0]; *(LAS float*)(L + L_MLP + wave * 32 + 16 + g * 4) = r2[1]; }
  }
  wg_barrier();
  if (tid < 16) {
    const int eh = tid >> 3, e = tid & 7;
    float y = 0.f;
#pragma unroll
    for (int w = 0; w < 4; ++w) y += *(LAS float*)(L + L_MLP + (eh * 4 + w) * 32 + e * 4);
    const float h = *(LAS float*)(L + L_T + 64 + tid * 4);
    const float o = h + (y + bf1(b2r));
    p.out[b * DM + e16 * 16 + tid] = (unsigned short)(pk_bf16(o, o) & 0xffffu);
  }
  if (wave == 0) STAMP(9);
  if (wave == 0) { wait_lgkm(); if (lane < 16) ((GAS u64*)(p.ws + WS_STAMP))[blockIdx.x * 16 + lane] = *(volatile LAS u64*)(L + L_END + 8 * lane); }
}

__global__ void dbg_print(const unsigned char* ws) {
  if (threadIdx.x != 0) return;
  const u64* h = (const u64*)(ws + WS_STAMP);
  u64 a = ~0ull; for (int c = 0; c < 256; ++c) a = h[c * 16] < a ? h[c * 16] : a;
  for (int i = 0; i < 16; ++i) { u64 mx = 0, mn = ~0ull; double sum = 0; for (int c = 0; c < 256; ++c) { u64 d = h[c * 16 + i] - a; mx = d > mx ? d : mx; mn = d < mn ? d : mn; sum += (double)d; }
    printf("stamp %d: min %.2f mean %.2f max %.2f\n", i, mn * 0.01, sum / 256 * 0.01, mx * 0.01); }
}
}

extern "C" void kernel_launch(void* const* d_in, const int* in_sizes, int n_in, void* d_out, int out_size, void* d_ws, size_t ws_size,
                              hipStream_t stream) {
  Params p;
  p.x = (const unsigned short*)d_in[0]; p.k = (const unsigned short*)d_in[1]; p.v = (const unsigned short*)d_in[2];
  p.wq = (const unsigned short*)d_in[3]; p.wo = (const unsigned short*)d_in[4];
  p.w1 = (const unsigned short*)d_in[6]; p.b1 = (const unsigned short*)d_in[7]; p.w2 = (const unsigned short*)d_in[8]; p.b2 = (const unsigned short*)d_in[9];
  p.out = (unsigned short*)d_out; p.ws = (unsigned char*)d_ws;
  (void)hipFuncSetAttribute((const void*)fused_decode_kernel, hipFuncAttributeMaxDynamicSharedMemorySize, (int)L_END + 128);
  hipLaunchKernelGGL(fused_decode_kernel, dim3(NWG), dim3(NTHR), L_END + 128, stream, p);
  hipLaunchKernelGGL(dbg_print, dim3(1), dim3(64), 0, stream, (const unsigned char*)d_ws);
}
```

```cpp
#define X_QDELAY 64
#include <hip/hip_runtime.h>
#include <hip/hip_bf16.h>
#include <stdint.h>

#define LAS __attribute__((address_space(3)))
#define GAS __attribute__((address_space(1)))
typedef unsigned v4u __attribute__((ext_vector_type(4)));
typedef unsigned v2u __attribute__((ext_vector_type(2)));
typedef int v4i __attribute__((ext_vector_type(4)));
typedef float f32x4 __attribute__((ext_vector_type(4)));
typedef float f32x2 __attribute__((ext_vector_type(2)));
typedef short s16x4 __attribute__((ext_vector_type(4)));
typedef __bf16 bf16x8 __attribute__((ext_vector_type(8)));
typedef __bf16 bf16x2 __attribute__((ext_vector_type(2)));
typedef unsigned long long u64;

#define FLY_PIECES 2
#define X_NOWQ 0
#define X_NOQST 0
#define X_NOGATHER 0
#define X_QSLEEP 32
#define X_H1START 2
#define X_KVFIRST 1
#define W_NT 0
#define STAMPS 0

namespace {

constexpr int NB = 8, DM = 512, HQ = 32, HKV = 8, DH = 128;
constexpr int NWG = 256, NTHR = 512;
constexpr int TILES = 32;
constexpr int FLY = FLY_PIECES;
static_assert(FLY >= 2 && FLY <= 4, "in-flight pieces");
constexpr float LN_EPS = 1e-5f;
constexpr float SCALE = 0.08838834764831845f, LOG2E = 1.4426950408889634f;

constexpr unsigned WS_EPOCH = 0;
constexpr unsigned WS_FLAG = 4096;
constexpr unsigned WS_Y = 8192;
constexpr unsigned WS_Q = WS_Y + NB * HKV * DM * 4;
constexpr unsigned REC_BYTES = 2304;
constexpr unsigned WS_REC = WS_Q + NB * 4096 * 8;
constexpr unsigned WS_STAMP = WS_REC + NWG * REC_BYTES;
constexpr unsigned WS_END = WS_STAMP + NWG * 16 * 8;

constexpr unsigned L_RING = 0;
constexpr unsigned L_STAGE = 131072;
constexpr unsigned L_QBUF = L_STAGE + 16384;
constexpr unsigned L_PART = L_QBUF + 1024;
constexpr unsigned L_XN = L_PART, XN_ROW = 1040;
constexpr unsigned L_ML = L_PART + 8320;
constexpr unsigned L_Y = L_ML + 128;
constexpr unsigned L_T = L_Y + 2048;
constexpr unsigned L_MLP = L_T + 128;
constexpr unsigned L_CTL = L_MLP + 256;
constexpr unsigned L_END = L_CTL + 128;
static_assert(L_END + 128 <= 160 * 1024, "LDS budget");
enum { C_QRDY = 0, C_XN = 1, C_MERGE = 2, C_YCNT = 3, C_TLO = 4, C_THI = 5, C_TRDY = 6, C_NEAR = 7, C_FLAGOK = 8, C_WO = 12   };

constexpr unsigned SPIN_MAX = 1u << 18;

struct Params {
  const unsigned short* k; const unsigned short* v; const unsigned short* wo; unsigned char* ws;
  const unsigned short* x; const unsigned short* wq;
  const unsigned short* w1; const unsigned short* b1; const unsigned short* w2; const unsigned short* b2;
  unsigned short* out;
};

__device__ __forceinline__ v4i mkrsrc(const void* p, unsigned bytes) {
  const u64 a = (u64)p;
  v4i r; r.x = __builtin_amdgcn_readfirstlane((int)(unsigned)a); r.y = __builtin_amdgcn_readfirstlane((int)((unsigned)(a >> 32) & 0xffffu));
  r.z = (int)bytes; r.w = 0x00020000; return r;
}
__device__ __forceinline__ void dma4_nt(v4i rs, unsigned v0, unsigned v1, unsigned v2, unsigned v3, unsigned soff, unsigned lds) {
  asm volatile("s_mov_b32 m0, %6\n\ts_nop 0\n\t"
               "buffer_load_dwordx4 %0, %4, %5 offen nt lds\n\t"
               "buffer_load_dwordx4 %1, %4, %5 offen offset:1024 nt lds\n\t"
               "buffer_load_dwordx4 %2, %4, %5 offen offset:2048 nt lds\n\t"
               "buffer_load_dwordx4 %3, %4, %5 offen offset:3072 nt lds"
               :: "v"(v0), "v"(v1), "v"(v2), "v"(v3), "s"(rs), "s"(soff), "s"(lds) : "memory");
}
#define WPOL ""
__device__ __forceinline__ void dma4_w(v4i rs, unsigned v0, unsigned v1, unsigned v2, unsigned v3, unsigned soff, unsigned lds) {
  asm volatile("s_mov_b32 m0, %6\n\ts_nop 0\n\t"
               "buffer_load_dwordx4 %0, %4, %5 offen" WPOL " lds\n\t"
               "buffer_load_dwordx4 %1, %4, %5 offen offset:1024" WPOL " lds\n\t"
               "buffer_load_dwordx4 %2, %4, %5 offen offset:2048" WPOL " lds\n\t"
               "buffer_load_dwordx4 %3, %4, %5 offen offset:3072" WPOL " lds"
               :: "v"(v0), "v"(v1), "v"(v2), "v"(v3), "s"(rs), "s"(soff), "s"(lds) : "memory");
}
template <int N> __device__ __forceinline__ void wait_vm() { asm volatile("s_waitcnt vmcnt(%0)" :: "i"(N) : "memory"); }
__device__ __forceinline__ void wait_lgkm() { asm volatile("s_waitcnt lgkmcnt(0)" ::: "memory"); }
__device__ __forceinline__ void wg_barrier() { asm volatile("s_waitcnt lgkmcnt(0)\n\ts_barrier" ::: "memory"); }

template <int CTRL> __device__ __forceinline__ float dpp(float x) {
  return __builtin_bit_cast(float, __builtin_amdgcn_mov_dpp(__builtin_bit_cast(int, x), CTRL, 0xf, 0xf, true));
}
constexpr int DPP_XOR1 = 0xB1, DPP_XOR2 = 0x4E, DPP_HMIRROR = 0x141, DPP_ROR8 = 0x128;
__device__ __forceinline__ float xrow_max(float x) {
  auto s = __builtin_amdgcn_permlane16_swap(__float_as_uint(x), __float_as_uint(x), false, false);
  x = fmaxf(__uint_as_float(s[0]), __uint_as_float(s[1]));
  auto t = __builtin_amdgcn_permlane32_swap(__float_as_uint(x), __float_as_uint(x), false, false);
  return fmaxf(__uint_as_float(t[0]), __uint_as_float(t[1]));
}
__device__ __forceinline__ float xrow_sum(float x) {
  auto s = __builtin_amdgcn_permlane16_swap(__float_as_uint(x), __float_as_uint(x), false, false);
  x = __uint_as_float(s[0]) + __uint_as_float(s[1]);
  auto t = __builtin_amdgcn_permlane32_swap(__float_as_uint(x), __float_as_uint(x), false, false);
  return __uint_as_float(t[0]) + __uint_as_float(t[1]);
}
__device__ __forceinline__ float row16_sum(float x) {
  x += dpp<DPP_XOR1>(x); x += dpp<DPP_XOR2>(x); x += dpp<DPP_HMIRROR>(x); x += dpp<DPP_ROR8>(x); return x;
}
__device__ __forceinline__ float wave_sum(float x) { return xrow_sum(row16_sum(x)); }
__device__ __forceinline__ float xrow_sum4(float a, float b, float c, float d) {
  auto s01 = __builtin_amdgcn_permlane16_swap(__float_as_uint(a), __float_as_uint(b), false, false);
  auto s23 = __builtin_amdgcn_permlane16_swap(__float_as_uint(c), __float_as_uint(d), false, false);
  const float x = __uint_as_float(s01[0]) + __uint_as_float(s01[1]), y = __uint_as_float(s23[0]) + __uint_as_float(s23[1]);
  auto t = __builtin_amdgcn_permlane32_swap(__float_as_uint(x), __float_as_uint(y), false, false);
  return __uint_as_float(t[0]) + __uint_as_float(t[1]);
}
__device__ __forceinline__ unsigned pk_bf16(float lo, float hi) {
  bf16x2 r; r[0] = (__bf16)lo; r[1] = (__bf16)hi; return __builtin_bit_cast(unsigned, r);
}
__device__ __forceinline__ float bf_lo(unsigned u) { return __uint_as_float(u << 16); }
__device__ __forceinline__ float bf_hi(unsigned u) { return __uint_as_float(u & 0xffff0000u); }
__device__ __forceinline__ float bf1(unsigned short u) { return __uint_as_float((unsigned)u << 16); }
__device__ __forceinline__ float rbf(float x) { return bf_lo(pk_bf16(x, x)); }
__device__ __forceinline__ f32x4 mfma32(v4u a, v4u b, f32x4 c) {
  return __builtin_amdgcn_mfma_f32_16x16x32_bf16(__builtin_bit_cast(bf16x8, a), __builtin_bit_cast(bf16x8, b), c, 0, 0, 0);
}
__device__ __forceinline__ f32x4 mfma16(v2u a, v2u b, f32x4 c) {
  return __builtin_amdgcn_mfma_f32_16x16x16bf16_1k(__builtin_bit_cast(s16x4, a), __builtin_bit_cast(s16x4, b), c, 0, 0, 0);
}
__device__ __forceinline__ v2u tr_read(LAS unsigned char* p) {
  return __builtin_bit_cast(v2u, __builtin_amdgcn_ds_read_tr16_b64_v4i16((LAS s16x4*)p));
}
__device__ __forceinline__ v4u ld_sc1(__amdgpu_buffer_rsrc_t rs, unsigned off) { return __builtin_amdgcn_raw_buffer_load_b128(rs, (int)off, 0, 16); }
__device__ __forceinline__ void st_sc1(__amdgpu_buffer_rsrc_t rs, unsigned off, v4u v) { __builtin_amdgcn_raw_buffer_store_b128(v, rs, (int)off, 0, 16); }
__device__ __forceinline__ unsigned lds_ld(LAS unsigned char* L, int idx) { return *(volatile LAS unsigned*)(L + L_CTL + 4 * idx); }
__device__ __forceinline__ void lds_st(LAS unsigned char* L, int idx, unsigned v) { *(volatile LAS unsigned*)(L + L_CTL + 4 * idx) = v; }
__device__ __forceinline__ void lds_wait(LAS unsigned char* L, int idx, unsigned want) {
  for (unsigned s = 0; s < SPIN_MAX; ++s) { if (lds_ld(L, idx) >= want) break; __builtin_amdgcn_s_sleep(1); }
  asm volatile("" ::: "memory");
}
__device__ __forceinline__ unsigned tagf(float f) { return __float_as_uint(f) | 1u; }
#define STAMP(i) do { } while (0)

struct AttnState { float m, l; f32x4 o[8]; };

constexpr int slot_of(int n) { return n < 24 ? (n & 7) : (n & 3); }
struct StreamCtx {
  LAS unsigned char* L; unsigned ring; v4i rk, rv; unsigned vk[4], vv[2], ka[4], va[8]; unsigned tbase;
  template <int M> __device__ __forceinline__ void issue(int j) const {
    if constexpr ((M & 1) == 0) dma4_nt(rk, vk[0], vk[1], vk[2], vk[3], tbase + (unsigned)j * 65536u, ring + slot_of(M) * 4096);
    else dma4_nt(rv, vv[0], vv[1], vv[0], vv[1], tbase + (unsigned)j * 65536u, ring + slot_of(M) * 4096);
  }
  template <int N> __device__ __forceinline__ void step(int t) const {
    wait_vm<(N + 1 < 2 * TILES) ? 4 : 0>();
    if constexpr (N + 2 < 2 * TILES) issue<N + 2>(t + 1);
  }
};

__device__ __forceinline__ void softmax_pv(AttnState& st, const f32x4 sc, const v2u (&vf)[8], bool lds_pending) {
  const unsigned r01 = pk_bf16(sc[0], sc[1]), r23 = pk_bf16(sc[2], sc[3]);
  const unsigned u01 = pk_bf16(bf_lo(r01) * SCALE, bf_hi(r01) * SCALE), u23 = pk_bf16(bf_lo(r23) * SCALE, bf_hi(r23) * SCALE);
  const float s0 = bf_lo(u01), s1 = bf_hi(u01), s2 = bf_lo(u23), s3 = bf_hi(u23);
  const float tm = xrow_max(fmaxf(fmaxf(s0, s1), fmaxf(s2, s3)));
  if (__builtin_amdgcn_ballot_w64(tm > st.m) != 0ull) {
    const float mn = fmaxf(st.m, tm);
    const float al = __builtin_amdgcn_exp2f((st.m - mn) * LOG2E);
    st.m = mn; st.l *= al;
    const float a0 = dpp<0x00>(al), a1 = dpp<0x55>(al), a2 = dpp<0xAA>(al), a3 = dpp<0xFF>(al);
#pragma unroll
    for (int c = 0; c < 8; ++c) { st.o[c][0] *= a0; st.o[c][1] *= a1; st.o[c][2] *= a2; st.o[c][3] *= a3; }
  }
  const float p0 = __builtin_amdgcn_exp2f((s0 - st.m) * LOG2E), p1 = __builtin_amdgcn_exp2f((s1 - st.m) * LOG2E);
  const float p2 = __builtin_amdgcn_exp2f((s2 - st.m) * LOG2E), p3 = __builtin_amdgcn_exp2f((s3 - st.m) * LOG2E);
  st.l += (p0 + p1) + (p2 + p3);
  v2u pf; pf.x = pk_bf16(p0, p1); pf.y = pk_bf16(p2, p3);
  if (lds_pending) wait_lgkm();
#pragma unroll
  for (int c = 0; c < 8; ++c) st.o[c] = mfma16(pf, vf[c], st.o[c]);
}
struct TileRegs { v4u kf[4]; v2u vf[8]; };
template <int TC> __device__ __forceinline__ void read_k(TileRegs& tr, const StreamCtx& cx) {
#pragma unroll
  for (int s = 0; s < 4; ++s) tr.kf[s] = *(LAS v4u*)(cx.L + cx.ring + slot_of(2 * TC) * 4096 + cx.ka[s]);
}
template <int TC> __device__ __forceinline__ void read_v(TileRegs& tr, const StreamCtx& cx) {
#pragma unroll
  for (int c = 0; c < 8; ++c) tr.vf[c] = tr_read(cx.L + cx.ring + slot_of(2 * TC) * 4096 + 4096 + cx.va[c]);
}
__device__ __forceinline__ f32x4 qk(const TileRegs& tr, const v4u (&qf)[4]) {
  f32x4 sc = {0.f, 0.f, 0.f, 0.f};
#pragma unroll
  for (int s = 0; s < 4; ++s) sc = mfma32(tr.kf[s], qf[s], sc);
  return sc;
}
template <int TC> __device__ __forceinline__ void attn_tile(AttnState& st, const StreamCtx& cx, const v4u (&qf)[4], int t) {
  TileRegs tr;
  cx.step<2 * TC>(t); read_k<TC>(tr, cx);
  const f32x4 sc = qk(tr, qf);
  cx.step<2 * TC + 1>(t); read_v<TC>(tr, cx);
  softmax_pv(st, sc, tr.vf, true);
}
template <int TC> __device__ __forceinline__ void ring_tile(AttnState& st, const StreamCtx& cx, const v4u (&qf)[4]) {
  TileRegs tr; read_k<TC>(tr, cx); read_v<TC>(tr, cx);
  softmax_pv(st, qk(tr, qf), tr.vf, true);
}
template <int TC> __device__ __forceinline__ void park_tile(TileRegs& tr, const StreamCtx& cx) {
  cx.step<2 * TC>(TC); read_k<TC>(tr, cx);
  cx.step<2 * TC + 1>(TC); read_v<TC>(tr, cx);
  wait_lgkm();
}
__device__ __forceinline__ void parked_tile(AttnState& st, const TileRegs& tr, const v4u (&qf)[4]) { softmax_pv(st, qk(tr, qf), tr.vf, false); }
__device__ __forceinline__ f32x2 rbf2(f32x2 v) { const unsigned u = pk_bf16(v.x, v.y); return f32x2{bf_lo(u), bf_hi(u)}; }
__device__ __forceinline__ void ln_stats(const f32x2 (&z)[4], float& mb, float& rs) {
  const float pv = __builtin_bit_cast(float, __builtin_amdgcn_readfirstlane(__builtin_bit_cast(int, z[0].x)));
  f32x2 s = {0.f, 0.f}, q = {0.f, 0.f};
#pragma unroll
  for (int e = 0; e < 4; ++e) { const f32x2 d = z[e] - f32x2{pv, pv}; s += d; q = __builtin_elementwise_fma(d, d, q); }
  float s1 = s.x + s.y, q1 = q.x + q.y;
  s1 += dpp<DPP_XOR1>(s1); q1 += dpp<DPP_XOR1>(q1); s1 += dpp<DPP_XOR2>(s1); q1 += dpp<DPP_XOR2>(q1);
  s1 += dpp<DPP_HMIRROR>(s1); q1 += dpp<DPP_HMIRROR>(q1); s1 += dpp<DPP_ROR8>(s1); q1 += dpp<DPP_ROR8>(q1);
  const float dm = xrow_sum(s1) * (1.f / DM), q2 = xrow_sum(q1) * (1.f / DM);
  const float var = fmaxf(fmaf(-dm, dm, q2), 0.f);
  mb = rbf(pv + dm);
  rs = __builtin_amdgcn_rcpf(rbf(__builtin_amdgcn_sqrtf(rbf(rbf(var) + LN_EPS))));
}
__device__ __forceinline__ f32x2 ln_apply(f32x2 z, float mb, float rs) { return rbf2(rbf2(z - f32x2{mb, mb}) * f32x2{rs, rs}); }

__global__ void __launch_bounds__(NTHR, 2) fused_decode_kernel(Params p) {
  extern __shared__ __attribute__((aligned(16))) unsigned char lds_raw[];
  LAS unsigned char* L = (LAS unsigned char*)lds_raw;
  const int tid = threadIdx.x, lane = tid & 63, wave = __builtin_amdgcn_readfirstlane(tid >> 6);
  const int c = blockIdx.x;
  const int b = c >> 5, kvh = c & 7, sp = (c >> 3) & 3, grp = b * 8 + kvh;
  const int rid = grp * 4 + sp;
  const int cq = (c & 7) * 32 + (c >> 3);
  const int e16 = c & 31;
  const int g = lane >> 4, c16 = lane & 15;
  const __amdgpu_buffer_rsrc_t rws = __builtin_amdgcn_make_buffer_rsrc(p.ws, 0, WS_END, 0x00020000);

  float k3[7], k2[7], k1[7], k0[7], wa[7], wb[7]; unsigned short b2r;
  auto load_params = [&]() {
    constexpr float K0 = -2.f * 0.7978845608028654f * LOG2E, K1 = K0 * 0.044715f;
    unsigned short w1r[7], b1r[7], w2r[7];
    const int f0 = tid & 255;
#pragma unroll
    for (int i = 0; i < 7; ++i) { w1r[i] = p.w1[f0 + 256 * i]; b1r[i] = p.b1[f0 + 256 * i]; w2r[i] = p.w2[f0 + 256 * i]; }
    b2r = p.b2[0];
#pragma unroll
    for (int i = 0; i < 7; ++i) {
      const float a = bf1(w1r[i]), bb = bf1(b1r[i]), w2 = bf1(w2r[i]);
      k3[i] = K1 * a * a * a; k2[i] = 3.f * K1 * a * a * bb; k1[i] = a * fmaf(3.f * K1 * bb, bb, K0); k0[i] = bb * fmaf(K1 * bb, bb, K0);
      wa[i] = w2 * a; wb[i] = w2 * bb;
      asm volatile("" : "+v"(k3[i]), "+v"(k2[i]), "+v"(k1[i]), "+v"(k0[i]), "+v"(wa[i]), "+v"(wb[i]));
    }
  };
  const unsigned ra = 256 * (4 * g + (c16 >> 2)) + 16 * (((lane >> 1) & 1) ^ (((4 * g + (c16 >> 2)) & 7) << 1)) + 8 * (lane & 1);

  if (wave < 4) {
    if (wave == 0) STAMP(0);
    StreamCtx cx;
    cx.L = L; cx.rk = mkrsrc(p.k, 1u << 27); cx.rv = mkrsrc(p.v, 1u << 27);
#pragma unroll
    for (int i = 0; i < 4; ++i) cx.vk[i] = g * 256 + 16 * (c16 ^ (4 * i + g));
#pragma unroll
    for (int i = 0; i < 2; ++i) cx.vv[i] = g * 256 + 16 * (c16 ^ (((4 * i + g) & 7) << 1));
    cx.ring = L_RING + wave * 32768;
    cx.tbase = (unsigned)grp * (2048u * 1024u) + (unsigned)(sp * 4 + wave) * 4096u;
    cx.issue<0>(0); cx.issue<1>(0);
    wg_barrier();
#pragma unroll
    for (int s = 0; s < 4; ++s) cx.ka[s] = 256 * c16 + 16 * ((4 * s + g) ^ c16);
#pragma unroll
    for (int cc = 0; cc < 8; ++cc) cx.va[cc] = ra ^ (unsigned)(cc << 5);
    TileRegs pk0, pk1, pk2, pk3;
    park_tile<0>(pk0, cx); park_tile<1>(pk1, cx); park_tile<2>(pk2, cx); park_tile<3>(pk3, cx);
    cx.step<8>(4); cx.step<9>(4); cx.step<10>(5);
    if (wave == 0) STAMP(12);
    lds_wait(L, C_QRDY, 1u);
    v4u qf[4];
#pragma unroll
    for (int s = 0; s < 4; ++s) qf[s] = *(LAS v4u*)(L + L_QBUF + (lane & 3) * 256 + s * 64 + g * 16);
    AttnState st; st.m = -INFINITY; st.l = 0.f;
#pragma unroll
    for (int cc = 0; cc < 8; ++cc) st.o[cc] = f32x4{0.f, 0.f, 0.f, 0.f};
    parked_tile(st, pk0, qf);
    cx.step<11>(5); parked_tile(st, pk1, qf);
    cx.step<12>(6); parked_tile(st, pk2, qf);
    cx.step<13>(6); parked_tile(st, pk3, qf);
    ring_tile<4>(st, cx, qf); cx.step<14>(7);
    cx.step<15>(7); ring_tile<5>(st, cx, qf);
    cx.step<16>(8); ring_tile<6>(st, cx, qf);
    cx.step<17>(8); ring_tile<7>(st, cx, qf);
    cx.step<18>(9); ring_tile<8>(st, cx, qf);
    cx.step<19>(9); ring_tile<9>(st, cx, qf);
    attn_tile<10>(st, cx, qf, 10); attn_tile<11>(st, cx, qf, 11);
    lds_st(L, C_WO + wave, 1u);
    if (wave == 0) STAMP(13);
    for (int j = 12; j < TILES - 2; j += 2) { attn_tile<12>(st, cx, qf, j); attn_tile<13>(st, cx, qf, j + 1); }
    attn_tile<30>(st, cx, qf, 30); attn_tile<31>(st, cx, qf, 31);
    if (wave == 0) STAMP(2);
    const float lsum = xrow_sum(st.l);
#pragma unroll
    for (int cc = 0; cc < 8; ++cc) {
      const float v = g == 0 ? st.o[cc][0] : g == 1 ? st.o[cc][1] : g == 2 ? st.o[cc][2] : st.o[cc][3];
      *(LAS float*)(L + L_PART + wave * 2048 + g * 512 + (16 * cc + c16) * 4) = v;
    }
    if (lane < 4) *(LAS v2u*)(L + L_ML + wave * 32 + lane * 8) = v2u{__float_as_uint(st.m), __float_as_uint(lsum)};
    wait_lgkm();
    unsigned old = 0;
    if (lane == 0) old = __hip_atomic_fetch_add((LAS unsigned*)(L + L_CTL + 4 * C_MERGE), 1u, __ATOMIC_RELAXED, __HIP_MEMORY_SCOPE_WORKGROUP);
    old = __builtin_amdgcn_readfirstlane(old);
    asm volatile("" ::: "memory");
    if (old == 3u) {
      float mw[4], lw[4];
#pragma unroll
      for (int w = 0; w < 4; ++w) { const v2u ml = *(LAS v2u*)(L + L_ML + w * 32 + g * 8); mw[w] = __uint_as_float(ml.x); lw[w] = __uint_as_float(ml.y); }
      const float M = fmaxf(fmaxf(mw[0], mw[1]), fmaxf(mw[2], mw[3]));
      float Ls = 0.f; f32x4 a0 = {0.f, 0.f, 0.f, 0.f}, a1 = {0.f, 0.f, 0.f, 0.f};
#pragma unroll
      for (int w = 0; w < 4; ++w) {
        const float f = __builtin_amdgcn_exp2f((mw[w] - M) * LOG2E);
        const f32x4 x0 = *(LAS f32x4*)(L + L_PART + w * 2048 + g * 512 + c16 * 16), x1 = *(LAS f32x4*)(L + L_PART + w * 2048 + g * 512 + 256 + c16 * 16);
        Ls = fmaf(f, lw[w], Ls); a0 += f * x0; a1 += f * x1;
      }
      const unsigned rec = WS_REC + (unsigned)rid * REC_BYTES;
      st_sc1(rws, rec + g * 512 + c16 * 16, v4u{tagf(a0[0]), tagf(a0[1]), tagf(a0[2]), tagf(a0[3])});
      st_sc1(rws, rec + g * 512 + 256 + c16 * 16, v4u{tagf(a1[0]), tagf(a1[1]), tagf(a1[2]), tagf(a1[3])});
      if (c16 == 0) __builtin_amdgcn_raw_buffer_store_b64(v2u{tagf(M), tagf(Ls)}, rws, (int)(rec + 2048 + g * 8), 0, 16);
      STAMP(3);
    }
    load_params();
  } else {
    const int hw = wave - 4;
    const unsigned yslot = WS_Y + (((unsigned)b * 8u + (unsigned)kvh) * 512u + (unsigned)sp * 128u) * 4u;
    if (hw == 0) {
      const v4i rq = mkrsrc(p.wq, DM * HQ * DH * 2);
      const unsigned vo = (lane >> 1) * 8192 + (lane & 1) * 16;
#pragma unroll
      for (int s4 = 0; s4 < 4; ++s4)
        dma4_w(rq, vo, vo + 32u * 8192u - 1024u, vo + 64u * 8192u - 2048u, vo + 96u * 8192u - 3072u, (unsigned)cq * 32u + (unsigned)s4 * (128u * 8192u), L_STAGE + s4 * 4096);
      const u64 ep = __hip_atomic_load((GAS u64*)(p.ws + WS_EPOCH), __ATOMIC_RELAXED, __HIP_MEMORY_SCOPE_AGENT);
      wg_barrier();
      const u64 T = ep + 0x9E3779B97F4A7C15ull;
      if (lane == 0) { lds_st(L, C_TLO, (unsigned)T); lds_st(L, C_THI, (unsigned)(T >> 32)); }
      wait_vm<0>();
      STAMP(1);
      lds_wait(L, C_XN, 3u);
      f32x4 d = {0.f, 0.f, 0.f, 0.f};
      const unsigned ta = (8 * g + (c16 >> 2)) * 32 + (lane & 3) * 8;
#pragma unroll
      for (int s = 0; s < 16; ++s) {
        const v2u lo = tr_read(L + L_STAGE + s * 1024 + ta), hi = tr_read(L + L_STAGE + s * 1024 + ta + 128);
        const v4u xf = *(LAS v4u*)(L + L_XN + (c16 & 7) * XN_ROW + s * 64 + g * 16);
        d = mfma32(v4u{lo.x, lo.y, hi.x, hi.y}, xf, d);
      }
      const unsigned p0 = pk_bf16(d[0], d[1]), p1 = pk_bf16(d[2], d[3]);
      const int src = (c16 + 16 * (2 * (c16 >> 3) + (g >> 1))) * 4;
      const unsigned q0 = (unsigned)__builtin_amdgcn_ds_bpermute(src, (int)p0), q1 = (unsigned)__builtin_amdgcn_ds_bpermute(src, (int)p1);
      const unsigned qp = (g & 1) ? q1 : q0;
      const unsigned tlo = (unsigned)T << 16, thi = (unsigned)(T >> 16);
      st_sc1(rws, WS_Q + ((unsigned)(c16 & 7) * 4096u + (unsigned)cq * 16u) * 8u + (4 * (c16 >> 3) + g) * 16u,
             v4u{(qp & 0xffffu) | tlo, thi, (qp >> 16) | tlo, thi});
      STAMP(4);
      const unsigned qoff = WS_Q + ((unsigned)b * 4096u + (unsigned)kvh * 512u) * 8u + lane * 16u;
      v4u a0, a1, a2, a3; unsigned bad = 1u;
      __builtin_amdgcn_s_sleep(X_QDELAY);
      for (unsigned s = 0; s < SPIN_MAX; ++s) {
        asm volatile("" ::: "memory");
        if (bad != 0u) {
          a0 = ld_sc1(rws, qoff); a1 = ld_sc1(rws, qoff + 1024); a2 = ld_sc1(rws, qoff + 2048); a3 = ld_sc1(rws, qoff + 3072);
          bad = (((a0.x ^ tlo) | (a0.z ^ tlo) | (a1.x ^ tlo) | (a1.z ^ tlo) | (a2.x ^ tlo) | (a2.z ^ tlo) | (a3.x ^ tlo) | (a3.z ^ tlo)) >> 16) |
                (a0.y ^ thi) | (a0.w ^ thi) | (a1.y ^ thi) | (a1.w ^ thi) | (a2.y ^ thi) | (a2.w ^ thi) | (a3.y ^ thi) | (a3.w ^ thi);
        }
        if (__builtin_amdgcn_ballot_w64(bad != 0u) == 0ull) break;
        __builtin_amdgcn_s_sleep(X_QSLEEP);
      }
      *(LAS unsigned*)(L + L_QBUF + lane * 4) = (a0.x & 0xffffu) | (a0.z << 16);
      *(LAS unsigned*)(L + L_QBUF + 256 + lane * 4) = (a1.x & 0xffffu) | (a1.z << 16);
      *(LAS unsigned*)(L + L_QBUF + 512 + lane * 4) = (a2.x & 0xffffu) | (a2.z << 16);
      *(LAS unsigned*)(L + L_QBUF + 768 + lane * 4) = (a3.x & 0xffffu) | (a3.z << 16);
      wait_lgkm();
      lds_st(L, C_QRDY, 1u);
      STAMP(5);
    } else if (hw != 3) {
      v4u xv[4];
#pragma unroll
      for (int i = 0; i < 4; ++i) xv[i] = *((const GAS v4u*)(p.x + ((hw - 1) * 4 + i) * DM) + lane);
      wg_barrier();
#pragma unroll
      for (int i = 0; i < 4; ++i) {
        f32x2 z[4]; float mb, rs; v4u o;
#pragma unroll
        for (int e = 0; e < 4; ++e) z[e] = f32x2{bf_lo(xv[i][e]), bf_hi(xv[i][e])};
        ln_stats(z, mb, rs);
#pragma unroll
        for (int e = 0; e < 4; ++e) { const f32x2 t = rbf2(z[e] - f32x2{mb, mb}) * f32x2{rs, rs}; o[e] = pk_bf16(t.x, t.y); }
        *(LAS v4u*)(L + L_XN + ((hw - 1) * 4 + i) * XN_ROW + lane * 16) = o;
      }
      wait_lgkm();
      if (lane == 0) __hip_atomic_fetch_add((LAS unsigned*)(L + L_CTL + 4 * C_XN), 1u, __ATOMIC_RELAXED, __HIP_MEMORY_SCOPE_WORKGROUP);
    } else {
      const v4u z = {0u, 0u, 0u, 0u};
      const unsigned rec = WS_REC + (unsigned)rid * REC_BYTES;
      st_sc1(rws, rec + lane * 16, z); st_sc1(rws, rec + 1024 + lane * 16, z);
      if (lane < 2) st_sc1(rws, rec + 2048 + lane * 16, z);
      if (lane < 32) st_sc1(rws, yslot + lane * 16, z);
      if (lane < 32) lds_st(L, lane, 0u);
      wg_barrier();
      wait_vm<0>();
      if (lane == 0) __hip_atomic_fetch_add((LAS unsigned*)(L + L_CTL + 4 * C_XN), 1u, __ATOMIC_RELAXED, __HIP_MEMORY_SCOPE_WORKGROUP);
      lds_wait(L, C_WO + 3, 1u);
      const u64 T48 = ((u64)lds_ld(L, C_TLO) | ((u64)lds_ld(L, C_THI) << 32)) << 16;
      const GAS u64* fp = (const GAS u64*)(p.ws + WS_Q) + b * 4096 + ((lane & 7) * 32 + 4 * b + ((lane >> 3) & 3)) * 16;
      for (unsigned s = 0; s < SPIN_MAX; ++s) {
        const u64 f = __hip_atomic_load(fp, __ATOMIC_RELAXED, __HIP_MEMORY_SCOPE_AGENT);
        if (__builtin_amdgcn_ballot_w64(((f ^ T48) >> 16) != 0ull) == 0ull) break;
        __builtin_amdgcn_s_sleep(64);
      }
      asm volatile("" ::: "memory");
      lds_st(L, C_FLAGOK, 1u);
    }
    lds_wait(L, C_WO + hw, 1u);
    v2u wf[8][8];
    {
      const v4i ro = mkrsrc(p.wo, HQ * DH * DM * 2);
      const unsigned stg = L_RING + hw * 32768 + 16384;
      const unsigned wbase = (unsigned)(kvh * 512 + hw * 128) * 1024u + (unsigned)sp * 256u;
      const unsigned w0 = g * 1024 + 16 * (c16 ^ (g << 1)), w1 = g * 1024 + 3072 + 16 * (c16 ^ ((4 + g) << 1));
#pragma unroll
      for (int rnd = 0; rnd < 2; ++rnd) {
#pragma unroll
        for (int q4 = 0; q4 < 4; ++q4) { const int ch = rnd * 4 + q4; dma4_w(ro, w0, w1, w0 + 6144, w1 + 6144, wbase + (unsigned)ch * 16384u, stg + q4 * 4096); }
        wait_vm<0>();
#pragma unroll
        for (int q4 = 0; q4 < 4; ++q4)
#pragma unroll
          for (int cc = 0; cc < 8; ++cc) wf[rnd * 4 + q4][cc] = tr_read(L + stg + q4 * 4096 + (ra ^ (unsigned)(cc << 5)));
        wait_lgkm();
      }
    }
    load_params();
    v2u xr0, xr1;
    xr0 = *(const GAS v2u*)(p.x + b * DM + lane * 4); xr1 = *(const GAS v2u*)(p.x + b * DM + 256 + lane * 4);
    lds_wait(L, C_FLAGOK, 1u);
    if (hw == 0) STAMP(6);
    lds_wait(L, C_MERGE, X_H1START);
    {
      const int sg = lane & 3, jq = (lane >> 2) & 3;
      const unsigned rec = WS_REC + (unsigned)(grp * 4 + sg) * REC_BYTES;
      const unsigned doff = rec + hw * 512 + (32 * jq + 4 * g) * 4;
      v4u d0, d1; v2u hd; unsigned ok = 0u;
      for (unsigned s = 0; s < SPIN_MAX; ++s) {
        asm volatile("" ::: "memory");
        if ((ok & 1u) == 0u) {
          hd = __builtin_amdgcn_raw_buffer_load_b64(rws, (int)(rec + 2048 + hw * 8), 0, 16);
          d0 = ld_sc1(rws, doff); d1 = ld_sc1(rws, doff + 64);
          ok = hd.x & hd.y & d0.x & d0.y & d0.z & d0.w & d1.x & d1.y & d1.z & d1.w;
        }
        if (__builtin_amdgcn_ballot_w64((ok & 1u) == 0u) == 0ull) break;
      }
      if (hw == 0) STAMP(11);
      const float mh = __uint_as_float(hd.x), lh = __uint_as_float(hd.y);
      float mx = fmaxf(mh, dpp<DPP_XOR1>(mh)); mx = fmaxf(mx, dpp<DPP_XOR2>(mx));
      const float e = __builtin_amdgcn_exp2f((mh - mx) * LOG2E);
      float lt = e * lh; lt += dpp<DPP_XOR1>(lt); lt += dpp<DPP_XOR2>(lt);
      const float w = e * __builtin_amdgcn_rcpf(lt);
      float ov[8];
#pragma unroll
      for (int i = 0; i < 4; ++i) { ov[i] = __uint_as_float(d0[i]) * w; ov[4 + i] = __uint_as_float(d1[i]) * w; }
#pragma unroll
      for (int i = 0; i < 8; ++i) { ov[i] += dpp<DPP_XOR1>(ov[i]); ov[i] += dpp<DPP_XOR2>(ov[i]); }
      const v4u af = {pk_bf16(ov[0], ov[1]), pk_bf16(ov[2], ov[3]), pk_bf16(ov[4], ov[5]), pk_bf16(ov[6], ov[7])};
      f32x4 acc[8];
#pragma unroll
      for (int cc = 0; cc < 8; ++cc) acc[cc] = f32x4{0.f, 0.f, 0.f, 0.f};
#pragma unroll
      for (int j = 0; j < 4; ++j) {
        const v4u aj = jq == j ? af : v4u{0u, 0u, 0u, 0u};
#pragma unroll
        for (int cc = 0; cc < 8; ++cc) acc[cc] = mfma32(aj, v4u{wf[2 * j][cc].x, wf[2 * j][cc].y, wf[2 * j + 1][cc].x, wf[2 * j + 1][cc].y}, acc[cc]);
      }
      *(LAS float*)(L + L_Y + hw * 512 + lane * 4) = xrow_sum4(acc[0][0], acc[1][0], acc[2][0], acc[3][0]);
      *(LAS float*)(L + L_Y + hw * 512 + 256 + lane * 4) = xrow_sum4(acc[4][0], acc[5][0], acc[6][0], acc[7][0]);
    }
    wait_lgkm();
    unsigned old = 0;
    if (lane == 0) old = __hip_atomic_fetch_add((LAS unsigned*)(L + L_CTL + 4 * C_YCNT), 1u, __ATOMIC_RELAXED, __HIP_MEMORY_SCOPE_WORKGROUP);
    old = __builtin_amdgcn_readfirstlane(old);
    asm volatile("" ::: "memory");
    if (old == 3u) {
      STAMP(7);
      if (lane < 32) {
        const f32x4 y0 = *(LAS f32x4*)(L + L_Y + lane * 16), y1 = *(LAS f32x4*)(L + L_Y + 512 + lane * 16);
        const f32x4 y2 = *(LAS f32x4*)(L + L_Y + 1024 + lane * 16), y3 = *(LAS f32x4*)(L + L_Y + 1536 + lane * 16);
        const f32x4 y = (y0 + y1) + (y2 + y3);
        st_sc1(rws, yslot + lane * 16, v4u{tagf(y[0]), tagf(y[1]), tagf(y[2]), tagf(y[3])});
      }
      const unsigned yoff = WS_Y + (unsigned)b * 16384u + lane * 16;
      v4u yv[16]; unsigned pend = 0xffffu;
      for (unsigned s = 0; s < SPIN_MAX; ++s) {
        asm volatile("" ::: "memory");
#pragma unroll
        for (int i = 0; i < 16; ++i) if ((pend >> i) & 1u) yv[i] = ld_sc1(rws, yoff + i * 1024);
#pragma unroll
        for (int i = 0; i < 16; ++i) if ((pend >> i) & 1u) {
          const unsigned ok = yv[i].x & yv[i].y & yv[i].z & yv[i].w;
          if (__builtin_amdgcn_ballot_w64((ok & 1u) == 0u) == 0ull) pend &= ~(1u << i);
        }
        if (pend == 0u) break;
        if (__builtin_popcount(pend) > 4) __builtin_amdgcn_s_sleep(8);
      }
      STAMP(14);
      if (c == 0 && lane == 0) {
        const u64 T = (u64)lds_ld(L, C_TLO) | ((u64)lds_ld(L, C_THI) << 32);
        __hip_atomic_store((GAS u64*)(p.ws + WS_EPOCH), T, __ATOMIC_RELAXED, __HIP_MEMORY_SCOPE_AGENT);
      }
      f32x2 hv[4], z[4];
#pragma unroll
      for (int hf = 0; hf < 2; ++hf) {
        f32x4 ys[8];
#pragma unroll
        for (int kk = 0; kk < 8; ++kk) ys[kk] = __builtin_bit_cast(f32x4, yv[2 * kk + hf]);
        const f32x4 y = ((ys[0] + ys[1]) + (ys[2] + ys[3])) + ((ys[4] + ys[5]) + (ys[6] + ys[7]));
        const v2u xr = hf ? xr1 : xr0;
        hv[2 * hf] = rbf2(f32x2{bf_lo(xr.x), bf_hi(xr.x)} + rbf2(f32x2{y[0], y[1]}));
        hv[2 * hf + 1] = rbf2(f32x2{bf_lo(xr.y), bf_hi(xr.y)} + rbf2(f32x2{y[2], y[3]}));
      }
      float mb, rs;
      ln_stats(hv, mb, rs);
#pragma unroll
      for (int e = 0; e < 4; ++e) z[e] = ln_apply(hv[e], mb, rs);
      ln_stats(z, mb, rs);
      const bool hi = e16 >= 16;
      const f32x2 za = ln_apply(hi ? z[2] : z[0], mb, rs), zb = ln_apply(hi ? z[3] : z[1], mb, rs);
      const f32x2 ha = hi ? hv[2] : hv[0], hb = hi ? hv[3] : hv[1];
      const int l0 = (4 * e16) & 63;
      if (lane >= l0 && lane < l0 + 4) {
        *(LAS f32x4*)(L + L_T + (lane - l0) * 16) = f32x4{za.x, za.y, zb.x, zb.y};
        *(LAS f32x4*)(L + L_T + 64 + (lane - l0) * 16) = f32x4{ha.x, ha.y, hb.x, hb.y};
      }
      STAMP(8);
    }
  }
  wg_barrier();
  {
    const int eh = wave >> 2;
    const f32x4 t0 = *(LAS f32x4*)(L + L_T + eh * 32), t1 = *(LAS f32x4*)(L + L_T + eh * 32 + 16);
    const f32x2 tp[4] = {f32x2{t0[0], t0[1]}, f32x2{t0[2], t0[3]}, f32x2{t1[0], t1[1]}, f32x2{t1[2], t1[3]}};
    f32x2 acc[4];
#pragma unroll
    for (int e = 0; e < 4; ++e) acc[e] = f32x2{0.f, 0.f};
#pragma unroll
    for (int i = 0; i < 7; ++i) {
#pragma unroll
      for (int e = 0; e < 4; ++e) {
        const f32x2 t = tp[e];
        f32x2 ar = __builtin_elementwise_fma(f32x2{k3[i], k3[i]}, t, f32x2{k2[i], k2[i]});
        ar = __builtin_elementwise_fma(ar, t, f32x2{k1[i], k1[i]});
        ar = __builtin_elementwise_fma(ar, t, f32x2{k0[i], k0[i]});
        const f32x2 wu = __builtin_elementwise_fma(f32x2{wa[i], wa[i]}, t, f32x2{wb[i], wb[i]});
        f32x2 d = {__builtin_amdgcn_exp2f(ar.x), __builtin_amdgcn_exp2f(ar.y)};
        d += f32x2{1.f, 1.f};
        const f32x2 r = {__builtin_amdgcn_rcpf(d.x), __builtin_amdgcn_rcpf(d.y)};
        acc[e] = __builtin_elementwise_fma(wu, r, acc[e]);
      }
    }
    const float r0 = row16_sum(xrow_sum4(acc[0].x, acc[0].y, acc[1].x, acc[1].y));
    const float r1 = row16_sum(xrow_sum4(acc[2].x, acc[2].y, acc[3].x, acc[3].y));
    if (c16 == 0) { *(LAS float*)(L + L_MLP + wave * 32 + g * 4) = r0; *(LAS float*)(L + L_MLP + wave * 32 + 16 + g * 4) = r1; }
  }
  if (wave == 7) STAMP(15);
  wg_barrier();
  if (tid < 16) {
    const int eh = tid >> 3, e = tid & 7;
    float y = 0.f;
#pragma unroll
    for (int w = 0; w < 4; ++w) y += *(LAS float*)(L + L_MLP + (eh * 4 + w) * 32 + e * 4);
    const float h = *(LAS float*)(L + L_T + 64 + tid * 4);
    const float o = h + rbf(rbf(y) + bf1(b2r));
    p.out[b * DM + e16 * 16 + tid] = (unsigned short)(pk_bf16(o, o) & 0xffffu);
  }
}

}

extern "C" void kernel_launch(void* const* d_in, const int* in_sizes, int n_in, void* d_out, int out_size, void* d_ws, size_t ws_size,
                              hipStream_t stream) {
  Params p;
  p.x = (const unsigned short*)d_in[0]; p.k = (const unsigned short*)d_in[1]; p.v = (const unsigned short*)d_in[2];
  p.wq = (const unsigned short*)d_in[3]; p.wo = (const unsigned short*)d_in[4];
  p.w1 = (const unsigned short*)d_in[6]; p.b1 = (const unsigned short*)d_in[7]; p.w2 = (const unsigned short*)d_in[8]; p.b2 = (const unsigned short*)d_in[9];
  p.out = (unsigned short*)d_out; p.ws = (unsigned char*)d_ws;
  (void)hipFuncSetAttribute((const void*)fused_decode_kernel, hipFuncAttributeMaxDynamicSharedMemorySize, (int)L_END + 128);
  hipLaunchKernelGGL(fused_decode_kernel, dim3(NWG), dim3(NTHR), L_END + 128, stream, p);
}
```

```cpp
#define X_NFIN 4
#include <hip/hip_runtime.h>
#include <hip/hip_bf16.h>
#include <stdint.h>

#define LAS __attribute__((address_space(3)))
#define GAS __attribute__((address_space(1)))
typedef unsigned v4u __attribute__((ext_vector_type(4)));
typedef unsigned v2u __attribute__((ext_vector_type(2)));
typedef int v4i __attribute__((ext_vector_type(4)));
typedef float f32x4 __attribute__((ext_vector_type(4)));
typedef float f32x2 __attribute__((ext_vector_type(2)));
typedef short s16x4 __attribute__((ext_vector_type(4)));
typedef __bf16 bf16x8 __attribute__((ext_vector_type(8)));
typedef __bf16 bf16x2 __attribute__((ext_vector_type(2)));
typedef unsigned long long u64;

#define FLY_PIECES 2
#define X_NOWQ 0
#define X_NOQST 0
#define X_NOGATHER 0
#define X_QSLEEP 32
#define X_H2SLEEP 1
#define X_PRIO 2
#define X_H1START 2
#define X_KVFIRST 1
#define X_QDELAY 64
#define W_NT 0
#define STAMPS 0

namespace {

constexpr int NB = 8, DM = 512, HQ = 32, HKV = 8, DH = 128;
constexpr int NWG = 256, NTHR = 512;
constexpr int TILES = 32;
constexpr int FLY = FLY_PIECES;
static_assert(FLY >= 2 && FLY <= 4, "in-flight pieces");
constexpr int NFIN = X_NFIN;
constexpr float LN_EPS = 1e-5f;
constexpr float SCALE = 0.08838834764831845f, LOG2E = 1.4426950408889634f;

constexpr unsigned WS_EPOCH = 0;
constexpr unsigned WS_FLAG = 4096;
constexpr unsigned WS_Y = 8192;
constexpr unsigned WS_Q = WS_Y + NB * HKV * DM * 4;
constexpr unsigned REC_BYTES = 2304;
constexpr unsigned WS_REC = WS_Q + NB * 4096 * 8;
constexpr unsigned WS_TAB = WS_REC + NWG * REC_BYTES;
constexpr unsigned WS_STAMP = WS_TAB + 8192 * 8;
constexpr unsigned WS_END = WS_STAMP + NWG * 16 * 8;

constexpr unsigned L_RING = 0;
constexpr unsigned L_STAGE = 131072;
constexpr unsigned L_QBUF = L_STAGE + 16384;
constexpr unsigned L_PART = L_QBUF + 1024;
constexpr unsigned L_XN = L_PART, XN_ROW = 1040;
constexpr unsigned L_ML = L_PART + 8320;
constexpr unsigned L_Y = L_ML + 128;
constexpr unsigned L_T = L_Y + 2048;
constexpr unsigned L_MLP = L_T + 128;
constexpr unsigned L_CTL = L_MLP + 512;
constexpr unsigned L_END = L_CTL + 128;
static_assert(L_END + 128 <= 160 * 1024, "LDS budget");
enum { C_QRDY = 0, C_XN = 1, C_MERGE = 2, C_YCNT = 3, C_TLO = 4, C_THI = 5, C_TRDY = 6, C_NEAR = 7, C_FLAGOK = 8, C_TCNT = 9, C_TABOK = 10, C_WO = 12   };

constexpr unsigned SPIN_MAX = 1u << 18;

struct Params {
  const unsigned short* k; const unsigned short* v; const unsigned short* wo; unsigned char* ws;
  const unsigned short* x; const unsigned short* wq;
  const unsigned short* w1; const unsigned short* b1; const unsigned short* w2; const unsigned short* b2;
  unsigned short* out;
};

__device__ __forceinline__ v4i mkrsrc(const void* p, unsigned bytes) {
  const u64 a = (u64)p;
  v4i r; r.x = __builtin_amdgcn_readfirstlane((int)(unsigned)a); r.y = __builtin_amdgcn_readfirstlane((int)((unsigned)(a >> 32) & 0xffffu));
  r.z = (int)bytes; r.w = 0x00020000; return r;
}
__device__ __forceinline__ void dma4_nt(v4i rs, unsigned v0, unsigned v1, unsigned v2, unsigned v3, unsigned soff, unsigned lds) {
  asm volatile("s_mov_b32 m0, %6\n\ts_nop 0\n\t"
               "buffer_load_dwordx4 %0, %4, %5 offen nt lds\n\t"
               "buffer_load_dwordx4 %1, %4, %5 offen offset:1024 nt lds\n\t"
               "buffer_load_dwordx4 %2, %4, %5 offen offset:2048 nt lds\n\t"
               "buffer_load_dwordx4 %3, %4, %5 offen offset:3072 nt lds"
               :: "v"(v0), "v"(v1), "v"(v2), "v"(v3), "s"(rs), "s"(soff), "s"(lds) : "memory");
}
#define WPOL ""
__device__ __forceinline__ void dma4_w(v4i rs, unsigned v0, unsigned v1, unsigned v2, unsigned v3, unsigned soff, unsigned lds) {
  asm volatile("s_mov_b32 m0, %6\n\ts_nop 0\n\t"
               "buffer_load_dwordx4 %0, %4, %5 offen" WPOL " lds\n\t"
               "buffer_load_dwordx4 %1, %4, %5 offen offset:1024" WPOL " lds\n\t"
               "buffer_load_dwordx4 %2, %4, %5 offen offset:2048" WPOL " lds\n\t"
               "buffer_load_dwordx4 %3, %4, %5 offen offset:3072" WPOL " lds"
               :: "v"(v0), "v"(v1), "v"(v2), "v"(v3), "s"(rs), "s"(soff), "s"(lds) : "memory");
}
template <int N> __device__ __forceinline__ void wait_vm() { asm volatile("s_waitcnt vmcnt(%0)" :: "i"(N) : "memory"); }
__device__ __forceinline__ void wait_lgkm() { asm volatile("s_waitcnt lgkmcnt(0)" ::: "memory"); }
__device__ __forceinline__ void wg_barrier() { asm volatile("s_waitcnt lgkmcnt(0)\n\ts_barrier" ::: "memory"); }

template <int CTRL> __device__ __forceinline__ float dpp(float x) {
  return __builtin_bit_cast(float, __builtin_amdgcn_mov_dpp(__builtin_bit_cast(int, x), CTRL, 0xf, 0xf, true));
}
constexpr int DPP_XOR1 = 0xB1, DPP_XOR2 = 0x4E, DPP_HMIRROR = 0x141, DPP_ROR8 = 0x128;
__device__ __forceinline__ float xrow_max(float x) {
  auto s = __builtin_amdgcn_permlane16_swap(__float_as_uint(x), __float_as_uint(x), false, false);
  x = fmaxf(__uint_as_float(s[0]), __uint_as_float(s[1]));
  auto t = __builtin_amdgcn_permlane32_swap(__float_as_uint(x), __float_as_uint(x), false, false);
  return fmaxf(__uint_as_float(t[0]), __uint_as_float(t[1]));
}
__device__ __forceinline__ float xrow_sum(float x) {
  auto s = __builtin_amdgcn_permlane16_swap(__float_as_uint(x), __float_as_uint(x), false, false);
  x = __uint_as_float(s[0]) + __uint_as_float(s[1]);
  auto t = __builtin_amdgcn_permlane32_swap(__float_as_uint(x), __float_as_uint(x), false, false);
  return __uint_as_float(t[0]) + __uint_as_float(t[1]);
}
__device__ __forceinline__ float row16_sum(float x) {
  x += dpp<DPP_XOR1>(x); x += dpp<DPP_XOR2>(x); x += dpp<DPP_HMIRROR>(x); x += dpp<DPP_ROR8>(x); return x;
}
__device__ __forceinline__ float wave_sum(float x) { return xrow_sum(row16_sum(x)); }
__device__ __forceinline__ float xrow_sum4(float a, float b, float c, float d) {
  auto s01 = __builtin_amdgcn_permlane16_swap(__float_as_uint(a), __float_as_uint(b), false, false);
  auto s23 = __builtin_amdgcn_permlane16_swap(__float_as_uint(c), __float_as_uint(d), false, false);
  const float x = __uint_as_float(s01[0]) + __uint_as_float(s01[1]), y = __uint_as_float(s23[0]) + __uint_as_float(s23[1]);
  auto t = __builtin_amdgcn_permlane32_swap(__float_as_uint(x), __float_as_uint(y), false, false);
  return __uint_as_float(t[0]) + __uint_as_float(t[1]);
}
__device__ __forceinline__ unsigned pk_bf16(float lo, float hi) {
  bf16x2 r; r[0] = (__bf16)lo; r[1] = (__bf16)hi; return __builtin_bit_cast(unsigned, r);
}
__device__ __forceinline__ float bf_lo(unsigned u) { return __uint_as_float(u << 16); }
__device__ __forceinline__ float bf_hi(unsigned u) { return __uint_as_float(u & 0xffff0000u); }
__device__ __forceinline__ float bf1(unsigned short u) { return __uint_as_float((unsigned)u << 16); }
__device__ __forceinline__ float rbf(float x) { return bf_lo(pk_bf16(x, x)); }
__device__ __forceinline__ f32x4 mfma32(v4u a, v4u b, f32x4 c) {
  return __builtin_amdgcn_mfma_f32_16x16x32_bf16(__builtin_bit_cast(bf16x8, a), __builtin_bit_cast(bf16x8, b), c, 0, 0, 0);
}
__device__ __forceinline__ f32x4 mfma16(v2u a, v2u b, f32x4 c) {
  return __builtin_amdgcn_mfma_f32_16x16x16bf16_1k(__builtin_bit_cast(s16x4, a), __builtin_bit_cast(s16x4, b), c, 0, 0, 0);
}
__device__ __forceinline__ v2u tr_read(LAS unsigned char* p) {
  return __builtin_bit_cast(v2u, __builtin_amdgcn_ds_read_tr16_b64_v4i16((LAS s16x4*)p));
}
__device__ __forceinline__ v4u ld_sc1(__amdgpu_buffer_rsrc_t rs, unsigned off) { return __builtin_amdgcn_raw_buffer_load_b128(rs, (int)off, 0, 16); }
__device__ __forceinline__ void st_sc1(__amdgpu_buffer_rsrc_t rs, unsigned off, v4u v) { __builtin_amdgcn_raw_buffer_store_b128(v, rs, (int)off, 0, 16); }
__device__ __forceinline__ unsigned lds_ld(LAS unsigned char* L, int idx) { return *(volatile LAS unsigned*)(L + L_CTL + 4 * idx); }
__device__ __forceinline__ void lds_st(LAS unsigned char* L, int idx, unsigned v) { *(volatile LAS unsigned*)(L + L_CTL + 4 * idx) = v; }
__device__ __forceinline__ void lds_wait(LAS unsigned char* L, int idx, unsigned want) {
  for (unsigned s = 0; s < SPIN_MAX; ++s) { if (lds_ld(L, idx) >= want) break; __builtin_amdgcn_s_sleep(1); }
  asm volatile("" ::: "memory");
}
__device__ __forceinline__ unsigned tagf(float f) { return __float_as_uint(f) | 1u; }
#define STAMP(i) do { } while (0)

struct AttnState { float m, l; f32x4 o[8]; };

constexpr int slot_of(int n) { return n < 24 ? (n & 7) : (n & 3); }
struct StreamCtx {
  LAS unsigned char* L; unsigned ring; v4i rk, rv; unsigned vk[4], vv[2], ka[4], va[8]; unsigned tbase;
  template <int M> __device__ __forceinline__ void issue(int j) const {
    if constexpr ((M & 1) == 0) dma4_nt(rk, vk[0], vk[1], vk[2], vk[3], tbase + (unsigned)j * 65536u, ring + slot_of(M) * 4096);
    else dma4_nt(rv, vv[0], vv[1], vv[0], vv[1], tbase + (unsigned)j * 65536u, ring + slot_of(M) * 4096);
  }
  template <int N> __device__ __forceinline__ void step(int t) const {
    wait_vm<(N + 1 < 2 * TILES) ? 4 : 0>();
    if constexpr (N + 2 < 2 * TILES) issue<N + 2>(t + 1);
  }
};

__device__ __forceinline__ void softmax_pv(AttnState& st, const f32x4 sc, const v2u (&vf)[8], bool lds_pending) {
  const unsigned r01 = pk_bf16(sc[0], sc[1]), r23 = pk_bf16(sc[2], sc[3]);
  const unsigned u01 = pk_bf16(bf_lo(r01) * SCALE, bf_hi(r01) * SCALE), u23 = pk_bf16(bf_lo(r23) * SCALE, bf_hi(r23) * SCALE);
  const float s0 = bf_lo(u01), s1 = bf_hi(u01), s2 = bf_lo(u23), s3 = bf_hi(u23);
  const float tm = xrow_max(fmaxf(fmaxf(s0, s1), fmaxf(s2, s3)));
  if (__builtin_amdgcn_ballot_w64(tm > st.m) != 0ull) {
    const float mn = fmaxf(st.m, tm);
    const float al = __builtin_amdgcn_exp2f((st.m - mn) * LOG2E);
    st.m = mn; st.l *= al;
    const float a0 = dpp<0x00>(al), a1 = dpp<0x55>(al), a2 = dpp<0xAA>(al), a3 = dpp<0xFF>(al);
#pragma unroll
    for (int c = 0; c < 8; ++c) { st.o[c][0] *= a0; st.o[c][1] *= a1; st.o[c][2] *= a2; st.o[c][3] *= a3; }
  }
  const float p0 = __builtin_amdgcn_exp2f((s0 - st.m) * LOG2E), p1 = __builtin_amdgcn_exp2f((s1 - st.m) * LOG2E);
  const float p2 = __builtin_amdgcn_exp2f((s2 - st.m) * LOG2E), p3 = __builtin_amdgcn_exp2f((s3 - st.m) * LOG2E);
  st.l += (p0 + p1) + (p2 + p3);
  v2u pf; pf.x = pk_bf16(p0, p1); pf.y = pk_bf16(p2, p3);
  if (lds_pending) wait_lgkm();
#pragma unroll
  for (int c = 0; c < 8; ++c) st.o[c] = mfma16(pf, vf[c], st.o[c]);
}
struct TileRegs { v4u kf[4]; v2u vf[8]; };
template <int TC> __device__ __forceinline__ void read_k(TileRegs& tr, const StreamCtx& cx) {
#pragma unroll
  for (int s = 0; s < 4; ++s) tr.kf[s] = *(LAS v4u*)(cx.L + cx.ring + slot_of(2 * TC) * 4096 + cx.ka[s]);
}
template <int TC> __device__ __forceinline__ void read_v(TileRegs& tr, const StreamCtx& cx) {
#pragma unroll
  for (int c = 0; c < 8; ++c) tr.vf[c] = tr_read(cx.L + cx.ring + slot_of(2 * TC) * 4096 + 4096 + cx.va[c]);
}
__device__ __forceinline__ f32x4 qk(const TileRegs& tr, const v4u (&qf)[4]) {
  f32x4 sc = {0.f, 0.f, 0.f, 0.f};
#pragma unroll
  for (int s = 0; s < 4; ++s) sc = mfma32(tr.kf[s], qf[s], sc);
  return sc;
}
template <int TC> __device__ __forceinline__ void attn_tile(AttnState& st, const StreamCtx& cx, const v4u (&qf)[4], int t) {
  TileRegs tr;
  cx.step<2 * TC>(t); read_k<TC>(tr, cx);
  const f32x4 sc = qk(tr, qf);
  cx.step<2 * TC + 1>(t); read_v<TC>(tr, cx);
  softmax_pv(st, sc, tr.vf, true);
}
template <int TC> __device__ __forceinline__ void ring_tile(AttnState& st, const StreamCtx& cx, const v4u (&qf)[4]) {
  TileRegs tr; read_k<TC>(tr, cx); read_v<TC>(tr, cx);
  softmax_pv(st, qk(tr, qf), tr.vf, true);
}
template <int TC> __device__ __forceinline__ void park_tile(TileRegs& tr, const StreamCtx& cx) {
  cx.step<2 * TC>(TC); read_k<TC>(tr, cx);
  cx.step<2 * TC + 1>(TC); read_v<TC>(tr, cx);
  wait_lgkm();
}
__device__ __forceinline__ void parked_tile(AttnState& st, const TileRegs& tr, const v4u (&qf)[4]) { softmax_pv(st, qk(tr, qf), tr.vf, false); }
__device__ __forceinline__ f32x2 rbf2(f32x2 v) { const unsigned u = pk_bf16(v.x, v.y); return f32x2{bf_lo(u), bf_hi(u)}; }
__device__ __forceinline__ void ln_stats(const f32x2 (&z)[4], float& mb, float& rs) {
  const float pv = __builtin_bit_cast(float, __builtin_amdgcn_readfirstlane(__builtin_bit_cast(int, z[0].x)));
  f32x2 s = {0.f, 0.f}, q = {0.f, 0.f};
#pragma unroll
  for (int e = 0; e < 4; ++e) { const f32x2 d = z[e] - f32x2{pv, pv}; s += d; q = __builtin_elementwise_fma(d, d, q); }
  float s1 = s.x + s.y, q1 = q.x + q.y;
  s1 += dpp<DPP_XOR1>(s1); q1 += dpp<DPP_XOR1>(q1); s1 += dpp<DPP_XOR2>(s1); q1 += dpp<DPP_XOR2>(q1);
  s1 += dpp<DPP_HMIRROR>(s1); q1 += dpp<DPP_HMIRROR>(q1); s1 += dpp<DPP_ROR8>(s1); q1 += dpp<DPP_ROR8>(q1);
  const float dm = xrow_sum(s1) * (1.f / DM), q2 = xrow_sum(q1) * (1.f / DM);
  const float var = fmaxf(fmaf(-dm, dm, q2), 0.f);
  mb = rbf(pv + dm);
  rs = __builtin_amdgcn_rcpf(rbf(__builtin_amdgcn_sqrtf(rbf(rbf(var) + LN_EPS))));
}
__device__ __forceinline__ f32x2 ln_apply(f32x2 z, float mb, float rs) { return rbf2(rbf2(z - f32x2{mb, mb}) * f32x2{rs, rs}); }

constexpr float GELU_K0 = -2.f * 0.7978845608028654f * LOG2E, GELU_K1 = GELU_K0 * 0.044715f;
struct MlpCoef { float k3, k2, k1, k0, wa, wb; };
__device__ __forceinline__ MlpCoef mlp_coef(unsigned short w1r, unsigned short b1r, unsigned short w2r) {
  constexpr float K0 = GELU_K0, K1 = GELU_K1;
  const float a = bf1(w1r), bb = bf1(b1r), w2 = bf1(w2r);
  MlpCoef c;
  c.k3 = K1 * a * a * a; c.k2 = 3.f * K1 * a * a * bb; c.k1 = a * fmaf(3.f * K1 * bb, bb, K0); c.k0 = bb * fmaf(K1 * bb, bb, K0);
  c.wa = w2 * a; c.wb = w2 * bb;
  return c;
}
__device__ __forceinline__ f32x2 mlp_term(const MlpCoef& c, f32x2 t, f32x2 acc) {
  f32x2 ar = __builtin_elementwise_fma(f32x2{c.k3, c.k3}, t, f32x2{c.k2, c.k2});
  ar = __builtin_elementwise_fma(ar, t, f32x2{c.k1, c.k1});
  ar = __builtin_elementwise_fma(ar, t, f32x2{c.k0, c.k0});
  const f32x2 wu = __builtin_elementwise_fma(f32x2{c.wa, c.wa}, t, f32x2{c.wb, c.wb});
  f32x2 d = {__builtin_amdgcn_exp2f(ar.x), __builtin_amdgcn_exp2f(ar.y)};
  d += f32x2{1.f, 1.f};
  const f32x2 r = {__builtin_amdgcn_rcpf(d.x), __builtin_amdgcn_rcpf(d.y)};
  return __builtin_elementwise_fma(wu, r, acc);
}
constexpr unsigned TAB_LO = 100u << 7, TAB_N = 4096u, TAB_ZERO = TAB_N - 1u;

__global__ void __launch_bounds__(NTHR, 2) fused_decode_kernel(Params p) {
  extern __shared__ __attribute__((aligned(16))) unsigned char lds_raw[];
  LAS unsigned char* L = (LAS unsigned char*)lds_raw;
  const int tid = threadIdx.x, lane = tid & 63, wave = __builtin_amdgcn_readfirstlane(tid >> 6);
  const int c = blockIdx.x;
  const int b = c >> 5, kvh = c & 7, sp = (c >> 3) & 3, grp = b * 8 + kvh;
  const int rid = grp * 4 + sp;
  const int cq = (c & 7) * 32 + (c >> 3);
  const int e16 = c & 31;
  const int g = lane >> 4, c16 = lane & 15;
  const __amdgpu_buffer_rsrc_t rws = __builtin_amdgcn_make_buffer_rsrc(p.ws, 0, WS_END, 0x00020000);

  const unsigned ra = 256 * (4 * g + (c16 >> 2)) + 16 * (((lane >> 1) & 1) ^ (((4 * g + (c16 >> 2)) & 7) << 1)) + 8 * (lane & 1);

  if (wave < 4) {
    if (wave == 0) STAMP(0);
    __builtin_amdgcn_s_setprio(X_PRIO);
    StreamCtx cx;
    cx.L = L; cx.rk = mkrsrc(p.k, 1u << 27); cx.rv = mkrsrc(p.v, 1u << 27);
#pragma unroll
    for (int i = 0; i < 4; ++i) cx.vk[i] = g * 256 + 16 * (c16 ^ (4 * i + g));
#pragma unroll
    for (int i = 0; i < 2; ++i) cx.vv[i] = g * 256 + 16 * (c16 ^ (((4 * i + g) & 7) << 1));
    cx.ring = L_RING + wave * 32768;
    cx.tbase = (unsigned)grp * (2048u * 1024u) + (unsigned)(sp * 4 + wave) * 4096u;
    cx.issue<0>(0); cx.issue<1>(0);
    wg_barrier();
#pragma unroll
    for (int s = 0; s < 4; ++s) cx.ka[s] = 256 * c16 + 16 * ((4 * s + g) ^ c16);
#pragma unroll
    for (int cc = 0; cc < 8; ++cc) cx.va[cc] = ra ^ (unsigned)(cc << 5);
    TileRegs pk0, pk1, pk2, pk3;
    park_tile<0>(pk0, cx); park_tile<1>(pk1, cx); park_tile<2>(pk2, cx); park_tile<3>(pk3, cx);
    cx.step<8>(4); cx.step<9>(4); cx.step<10>(5);
    if (wave == 0) STAMP(12);
    lds_wait(L, C_QRDY, 1u);
    v4u qf[4];
#pragma unroll
    for (int s = 0; s < 4; ++s) qf[s] = *(LAS v4u*)(L + L_QBUF + (lane & 3) * 256 + s * 64 + g * 16);
    AttnState st; st.m = -INFINITY; st.l = 0.f;
#pragma unroll
    for (int cc = 0; cc < 8; ++cc) st.o[cc] = f32x4{0.f, 0.f, 0.f, 0.f};
    parked_tile(st, pk0, qf);
    cx.step<11>(5); parked_tile(st, pk1, qf);
    cx.step<12>(6); parked_tile(st, pk2, qf);
    cx.step<13>(6); parked_tile(st, pk3, qf);
    ring_tile<4>(st, cx, qf); cx.step<14>(7);
    cx.step<15>(7); ring_tile<5>(st, cx, qf);
    cx.step<16>(8); ring_tile<6>(st, cx, qf);
    cx.step<17>(8); ring_tile<7>(st, cx, qf);
    cx.step<18>(9); ring_tile<8>(st, cx, qf);
    cx.step<19>(9); ring_tile<9>(st, cx, qf);
    attn_tile<10>(st, cx, qf, 10); attn_tile<11>(st, cx, qf, 11);
    lds_st(L, C_WO + wave, 1u);
    if (wave == 0) STAMP(13);
    for (int j = 12; j < TILES - 2; j += 2) { attn_tile<12>(st, cx, qf, j); attn_tile<13>(st, cx, qf, j + 1); }
    attn_tile<30>(st, cx, qf, 30); attn_tile<31>(st, cx, qf, 31);
    if (wave == 0) STAMP(2);
    const float lsum = xrow_sum(st.l);
#pragma unroll
    for (int cc = 0; cc < 8; ++cc) {
      const float v = g == 0 ? st.o[cc][0] : g == 1 ? st.o[cc][1] : g == 2 ? st.o[cc][2] : st.o[cc][3];
      *(LAS float*)(L + L_PART + wave * 2048 + g * 512 + (16 * cc + c16) * 4) = v;
    }
    if (lane < 4) *(LAS v2u*)(L + L_ML + wave * 32 + lane * 8) = v2u{__float_as_uint(st.m), __float_as_uint(lsum)};
    wait_lgkm();
    unsigned old = 0;
    if (lane == 0) old = __hip_atomic_fetch_add((LAS unsigned*)(L + L_CTL + 4 * C_MERGE), 1u, __ATOMIC_RELAXED, __HIP_MEMORY_SCOPE_WORKGROUP);
    old = __builtin_amdgcn_readfirstlane(old);
    asm volatile("" ::: "memory");
    if (old == 3u) {
      float mw[4], lw[4];
#pragma unroll
      for (int w = 0; w < 4; ++w) { const v2u ml = *(LAS v2u*)(L + L_ML + w * 32 + g * 8); mw[w] = __uint_as_float(ml.x); lw[w] = __uint_as_float(ml.y); }
      const float M = fmaxf(fmaxf(mw[0], mw[1]), fmaxf(mw[2], mw[3]));
      float Ls = 0.f; f32x4 a0 = {0.f, 0.f, 0.f, 0.f}, a1 = {0.f, 0.f, 0.f, 0.f};
#pragma unroll
      for (int w = 0; w < 4; ++w) {
        const float f = __builtin_amdgcn_exp2f((mw[w] - M) * LOG2E);
        const f32x4 x0 = *(LAS f32x4*)(L + L_PART + w * 2048 + g * 512 + c16 * 16), x1 = *(LAS f32x4*)(L + L_PART + w * 2048 + g * 512 + 256 + c16 * 16);
        Ls = fmaf(f, lw[w], Ls); a0 += f * x0; a1 += f * x1;
      }
      const unsigned rec = WS_REC + (unsigned)rid * REC_BYTES;
      st_sc1(rws, rec + g * 512 + c16 * 16, v4u{tagf(a0[0]), tagf(a0[1]), tagf(a0[2]), tagf(a0[3])});
      st_sc1(rws, rec + g * 512 + 256 + c16 * 16, v4u{tagf(a1[0]), tagf(a1[1]), tagf(a1[2]), tagf(a1[3])});
      if (c16 == 0) __builtin_amdgcn_raw_buffer_store_b64(v2u{tagf(M), tagf(Ls)}, rws, (int)(rec + 2048 + g * 8), 0, 16);
      STAMP(3);
    }
    if (wave == 0 && (e16 % (32 / NFIN)) == 0) {
      const unsigned short b2r = p.b2[0];
      const v2u xr0 = *(const GAS v2u*)(p.x + b * DM + lane * 4), xr1 = *(const GAS v2u*)(p.x + b * DM + 256 + lane * 4);
      lds_wait(L, C_FLAGOK, 1u);
      lds_wait(L, C_TABOK, 1u);
      const unsigned tl = lds_ld(L, C_TLO), th = lds_ld(L, C_THI);
      const unsigned yoff = WS_Y + (unsigned)b * 16384u + lane * 16;
      v4u yv[16]; unsigned pend = 0xffffu;
      for (unsigned s = 0; s < SPIN_MAX; ++s) {
        asm volatile("" ::: "memory");
#pragma unroll
        for (int i = 0; i < 16; ++i) if ((pend >> i) & 1u) yv[i] = ld_sc1(rws, yoff + i * 1024);
#pragma unroll
        for (int i = 0; i < 16; ++i) if ((pend >> i) & 1u) {
          const unsigned ok = yv[i].x & yv[i].y & yv[i].z & yv[i].w;
          if (__builtin_amdgcn_ballot_w64((ok & 1u) == 0u) == 0ull) pend &= ~(1u << i);
        }
        if (pend == 0u) break;
        if (__builtin_popcount(pend) > 4) __builtin_amdgcn_s_sleep(X_H2SLEEP);
      }
      STAMP(14);
      f32x2 hv[4], z[4];
#pragma unroll
      for (int hf = 0; hf < 2; ++hf) {
        f32x4 ys[8];
#pragma unroll
        for (int kk = 0; kk < 8; ++kk) ys[kk] = __builtin_bit_cast(f32x4, yv[2 * kk + hf]);
        const f32x4 y = ((ys[0] + ys[1]) + (ys[2] + ys[3])) + ((ys[4] + ys[5]) + (ys[6] + ys[7]));
        const v2u xr = hf ? xr1 : xr0;
        hv[2 * hf] = rbf2(f32x2{bf_lo(xr.x), bf_hi(xr.x)} + rbf2(f32x2{y[0], y[1]}));
        hv[2 * hf + 1] = rbf2(f32x2{bf_lo(xr.y), bf_hi(xr.y)} + rbf2(f32x2{y[2], y[3]}));
      }
      float mb, rs;
      ln_stats(hv, mb, rs);
#pragma unroll
      for (int e = 0; e < 4; ++e) z[e] = ln_apply(hv[e], mb, rs);
      ln_stats(z, mb, rs);
      STAMP(8);
      const float b2f = bf1(b2r);
#pragma unroll
      for (int hf = 0; hf < 2; ++hf) {
        if (NFIN > 1 && (e16 >> 4) != hf) continue;
        constexpr int NL = NFIN >= 2 ? 128 / NFIN : 64;
        const int l0 = NFIN >= 2 ? (4 * e16) & 63 : 0;
        const bool mine = lane >= l0 && lane < l0 + NL;
        const f32x2 za = ln_apply(z[2 * hf], mb, rs), zb = ln_apply(z[2 * hf + 1], mb, rs);
        const float tz[4] = {za.x, za.y, zb.x, zb.y}, h4[4] = {hv[2 * hf].x, hv[2 * hf].y, hv[2 * hf + 1].x, hv[2 * hf + 1].y};
        unsigned idx[4]; bool oot[4];
#pragma unroll
        for (int e = 0; e < 4; ++e) {
          const unsigned bits = __float_as_uint(tz[e]) >> 16, mag = bits & 0x7fffu;
          const unsigned rel = mag == 0u ? TAB_ZERO : mag - TAB_LO;
          const bool outside = mag != 0u && rel >= TAB_ZERO;
          oot[e] = mine && outside;
          idx[e] = outside ? 0u : rel * 2u + (bits >> 15);
        }
        float y0 = bf1(*(LAS unsigned short*)(L + L_STAGE + idx[0] * 2u)), y1 = bf1(*(LAS unsigned short*)(L + L_STAGE + idx[1] * 2u));
        float y2 = bf1(*(LAS unsigned short*)(L + L_STAGE + idx[2] * 2u)), y3 = bf1(*(LAS unsigned short*)(L + L_STAGE + idx[3] * 2u));
        if (__builtin_amdgcn_ballot_w64(oot[0] || oot[1] || oot[2] || oot[3]) != 0ull) {
#pragma nounroll
          for (int e = 0; e < 4; ++e) {
            const float te = e == 0 ? tz[0] : e == 1 ? tz[1] : e == 2 ? tz[2] : tz[3];
            u64 todo = __builtin_amdgcn_ballot_w64(e == 0 ? oot[0] : e == 1 ? oot[1] : e == 2 ? oot[2] : oot[3]);
            while (todo != 0ull) {
              const int src = __builtin_ctzll(todo); todo &= todo - 1ull;
              const float tv = __builtin_bit_cast(float, __builtin_amdgcn_readlane(__builtin_bit_cast(int, te), src));
              float acc = 0.f;
#pragma nounroll
              for (int i = 0; i < 28; ++i) {
                const float u = fmaf(tv, bf1(p.w1[lane + 64 * i]), bf1(p.b1[lane + 64 * i]));
                const float ex = __builtin_amdgcn_exp2f(u * fmaf(u * u, GELU_K1, GELU_K0));
                acc = fmaf(u * bf1(p.w2[lane + 64 * i]), __builtin_amdgcn_rcpf(1.f + ex), acc);
              }
              const float yd = rbf(wave_sum(acc));
              if (lane == src) { if (e == 0) y0 = yd; else if (e == 1) y1 = yd; else if (e == 2) y2 = yd; else y3 = yd; }
            }
          }
        }
        if (mine) {
          const v2u o = {pk_bf16(h4[0] + rbf(y0 + b2f), h4[1] + rbf(y1 + b2f)), pk_bf16(h4[2] + rbf(y2 + b2f), h4[3] + rbf(y3 + b2f))};
          *(GAS v2u*)(p.out + b * DM + hf * 256 + lane * 4) = o;
        }
      }
      if (c == 0 && lane == 0) __hip_atomic_store((GAS u64*)(p.ws + WS_EPOCH), (u64)tl | ((u64)th << 32), __ATOMIC_RELAXED, __HIP_MEMORY_SCOPE_AGENT);
      STAMP(9);
    }
  } else {
    const int hw = wave - 4;
    const unsigned yslot = WS_Y + (((unsigned)b * 8u + (unsigned)kvh) * 512u + (unsigned)sp * 128u) * 4u;
    if (hw == 0) {
      const v4i rq = mkrsrc(p.wq, DM * HQ * DH * 2);
      const unsigned vo = (lane >> 1) * 8192 + (lane & 1) * 16;
#pragma unroll
      for (int s4 = 0; s4 < 4; ++s4)
        dma4_w(rq, vo, vo + 32u * 8192u - 1024u, vo + 64u * 8192u - 2048u, vo + 96u * 8192u - 3072u, (unsigned)cq * 32u + (unsigned)s4 * (128u * 8192u), L_STAGE + s4 * 4096);
      const u64 ep = __hip_atomic_load((GAS u64*)(p.ws + WS_EPOCH), __ATOMIC_RELAXED, __HIP_MEMORY_SCOPE_AGENT);
      wg_barrier();
      const u64 T = ep + 0x9E3779B97F4A7C15ull;
      if (lane == 0) { lds_st(L, C_TLO, (unsigned)T); lds_st(L, C_THI, (unsigned)(T >> 32)); }
      wait_vm<0>();
      STAMP(1);
      lds_wait(L, C_XN, 3u);
      f32x4 d = {0.f, 0.f, 0.f, 0.f};
      const unsigned ta = (8 * g + (c16 >> 2)) * 32 + (lane & 3) * 8;
#pragma unroll
      for (int s = 0; s < 16; ++s) {
        const v2u lo = tr_read(L + L_STAGE + s * 1024 + ta), hi = tr_read(L + L_STAGE + s * 1024 + ta + 128);
        const v4u xf = *(LAS v4u*)(L + L_XN + (c16 & 7) * XN_ROW + s * 64 + g * 16);
        d = mfma32(v4u{lo.x, lo.y, hi.x, hi.y}, xf, d);
      }
      const unsigned p0 = pk_bf16(d[0], d[1]), p1 = pk_bf16(d[2], d[3]);
      const int src = (c16 + 16 * (2 * (c16 >> 3) + (g >> 1))) * 4;
      const unsigned q0 = (unsigned)__builtin_amdgcn_ds_bpermute(src, (int)p0), q1 = (unsigned)__builtin_amdgcn_ds_bpermute(src, (int)p1);
      const unsigned qp = (g & 1) ? q1 : q0;
      const unsigned tlo = (unsigned)T << 16, thi = (unsigned)(T >> 16);
      st_sc1(rws, WS_Q + ((unsigned)(c16 & 7) * 4096u + (unsigned)cq * 16u) * 8u + (4 * (c16 >> 3) + g) * 16u,
             v4u{(qp & 0xffffu) | tlo, thi, (qp >> 16) | tlo, thi});
      STAMP(4);
      const unsigned qoff = WS_Q + ((unsigned)b * 4096u + (unsigned)kvh * 512u) * 8u + lane * 16u;
      v4u a0, a1, a2, a3; unsigned bad = 1u;
      __builtin_amdgcn_s_sleep(X_QDELAY);
      for (unsigned s = 0; s < SPIN_MAX; ++s) {
        asm volatile("" ::: "memory");
        if (bad != 0u) {
          a0 = ld_sc1(rws, qoff); a1 = ld_sc1(rws, qoff + 1024); a2 = ld_sc1(rws, qoff + 2048); a3 = ld_sc1(rws, qoff + 3072);
          bad = (((a0.x ^ tlo) | (a0.z ^ tlo) | (a1.x ^ tlo) | (a1.z ^ tlo) | (a2.x ^ tlo) | (a2.z ^ tlo) | (a3.x ^ tlo) | (a3.z ^ tlo)) >> 16) |
                (a0.y ^ thi) | (a0.w ^ thi) | (a1.y ^ thi) | (a1.w ^ thi) | (a2.y ^ thi) | (a2.w ^ thi) | (a3.y ^ thi) | (a3.w ^ thi);
        }
        if (__builtin_amdgcn_ballot_w64(bad != 0u) == 0ull) break;
        __builtin_amdgcn_s_sleep(X_QSLEEP);
      }
      *(LAS unsigned*)(L + L_QBUF + lane * 4) = (a0.x & 0xffffu) | (a0.z << 16);
      *(LAS unsigned*)(L + L_QBUF + 256 + lane * 4) = (a1.x & 0xffffu) | (a1.z << 16);
      *(LAS unsigned*)(L + L_QBUF + 512 + lane * 4) = (a2.x & 0xffffu) | (a2.z << 16);
      *(LAS unsigned*)(L + L_QBUF + 768 + lane * 4) = (a3.x & 0xffffu) | (a3.z << 16);
      wait_lgkm();
      lds_st(L, C_QRDY, 1u);
      STAMP(5);
    } else if (hw != 3) {
      v4u xv[4];
#pragma unroll
      for (int i = 0; i < 4; ++i) xv[i] = *((const GAS v4u*)(p.x + ((hw - 1) * 4 + i) * DM) + lane);
      wg_barrier();
#pragma unroll
      for (int i = 0; i < 4; ++i) {
        f32x2 z[4]; float mb, rs; v4u o;
#pragma unroll
        for (int e = 0; e < 4; ++e) z[e] = f32x2{bf_lo(xv[i][e]), bf_hi(xv[i][e])};
        ln_stats(z, mb, rs);
#pragma unroll
        for (int e = 0; e < 4; ++e) { const f32x2 t = rbf2(z[e] - f32x2{mb, mb}) * f32x2{rs, rs}; o[e] = pk_bf16(t.x, t.y); }
        *(LAS v4u*)(L + L_XN + ((hw - 1) * 4 + i) * XN_ROW + lane * 16) = o;
      }
      wait_lgkm();
      if (lane == 0) __hip_atomic_fetch_add((LAS unsigned*)(L + L_CTL + 4 * C_XN), 1u, __ATOMIC_RELAXED, __HIP_MEMORY_SCOPE_WORKGROUP);
    } else {
      const v4u z = {0u, 0u, 0u, 0u};
      const unsigned rec = WS_REC + (unsigned)rid * REC_BYTES;
      st_sc1(rws, rec + lane * 16, z); st_sc1(rws, rec + 1024 + lane * 16, z);
      if (lane < 2) st_sc1(rws, rec + 2048 + lane * 16, z);
      if (lane < 32) st_sc1(rws, yslot + lane * 16, z);
      if (lane < 32) lds_st(L, lane, 0u);
      wg_barrier();
      wait_vm<0>();
      if (lane == 0) __hip_atomic_fetch_add((LAS unsigned*)(L + L_CTL + 4 * C_XN), 1u, __ATOMIC_RELAXED, __HIP_MEMORY_SCOPE_WORKGROUP);
    }
    {
      MlpCoef cf[7];
      const int f0 = tid & 255;
#pragma unroll
      for (int i = 0; i < 7; ++i) cf[i] = mlp_coef(p.w1[f0 + 256 * i], p.b1[f0 + 256 * i], p.w2[f0 + 256 * i]);
#pragma nounroll
      for (int ch = 0; ch < 4; ++ch) {
        f32x2 acc[4], tp[4];
#pragma unroll
        for (int e = 0; e < 4; ++e) {
          const unsigned mag = (unsigned)(c * 16 + ch * 4 + e);
          const float tv = mag == TAB_ZERO ? 0.f : __uint_as_float((TAB_LO + mag) << 16);
          tp[e] = f32x2{tv, -tv}; acc[e] = f32x2{0.f, 0.f};
        }
#pragma unroll
        for (int i = 0; i < 7; ++i)
#pragma unroll
          for (int e = 0; e < 4; ++e) acc[e] = mlp_term(cf[i], tp[e], acc[e]);
        const float r0 = row16_sum(xrow_sum4(acc[0].x, acc[0].y, acc[1].x, acc[1].y));
        const float r1 = row16_sum(xrow_sum4(acc[2].x, acc[2].y, acc[3].x, acc[3].y));
        if (c16 == 0) { *(LAS float*)(L + L_MLP + hw * 128 + ch * 32 + g * 4) = r0; *(LAS float*)(L + L_MLP + hw * 128 + ch * 32 + 16 + g * 4) = r1; }
      }
      wait_lgkm();
      unsigned old = 0;
      if (lane == 0) old = __hip_atomic_fetch_add((LAS unsigned*)(L + L_CTL + 4 * C_TCNT), 1u, __ATOMIC_RELAXED, __HIP_MEMORY_SCOPE_WORKGROUP);
      old = __builtin_amdgcn_readfirstlane(old);
      asm volatile("" ::: "memory");
      if (old == 3u && lane < 16) {
        f32x2 y = {0.f, 0.f};
#pragma unroll
        for (int w = 0; w < 4; ++w) y += *(LAS f32x2*)(L + L_MLP + w * 128 + lane * 8);
        const unsigned yp = pk_bf16(y.x, y.y), tl = lds_ld(L, C_TLO), th = lds_ld(L, C_THI);
        const unsigned tlo = tl << 16, thi = (tl >> 16) | (th << 16);
        st_sc1(rws, WS_TAB + (unsigned)c * 256u + lane * 16u, v4u{(yp & 0xffffu) | tlo, thi, (yp >> 16) | tlo, thi});
      }
    }
    if (hw == 0) STAMP(10);
    lds_wait(L, C_WO + hw, 1u);
    v2u wf[8][8];
    {
      const v4i ro = mkrsrc(p.wo, HQ * DH * DM * 2);
      const unsigned stg = L_RING + hw * 32768 + 16384;
      const unsigned wbase = (unsigned)(kvh * 512 + hw * 128) * 1024u + (unsigned)sp * 256u;
      const unsigned w0 = g * 1024 + 16 * (c16 ^ (g << 1)), w1 = g * 1024 + 3072 + 16 * (c16 ^ ((4 + g) << 1));
#pragma unroll
      for (int rnd = 0; rnd < 2; ++rnd) {
#pragma unroll
        for (int q4 = 0; q4 < 4; ++q4) { const int ch = rnd * 4 + q4; dma4_w(ro, w0, w1, w0 + 6144, w1 + 6144, wbase + (unsigned)ch * 16384u, stg + q4 * 4096); }
        wait_vm<0>();
#pragma unroll
        for (int q4 = 0; q4 < 4; ++q4)
#pragma unroll
          for (int cc = 0; cc < 8; ++cc) wf[rnd * 4 + q4][cc] = tr_read(L + stg + q4 * 4096 + (ra ^ (unsigned)(cc << 5)));
        wait_lgkm();
      }
    }
    if (hw == 1 && (e16 % (32 / NFIN)) == 0) {
      const unsigned tl = lds_ld(L, C_TLO), th = lds_ld(L, C_THI);
      const unsigned tlo = tl << 16, thi = (tl >> 16) | (th << 16);
#pragma nounroll
      for (int bt = 0; bt < 8; ++bt) {
        v4u tv[8];
        for (unsigned s = 0; s < SPIN_MAX; ++s) {
          asm volatile("" ::: "memory");
          unsigned bad = 0u;
#pragma unroll
          for (int i = 0; i < 8; ++i) tv[i] = ld_sc1(rws, WS_TAB + (unsigned)(bt * 8 + i) * 1024u + lane * 16u);
#pragma unroll
          for (int i = 0; i < 8; ++i) bad |= (((tv[i].x ^ tlo) | (tv[i].z ^ tlo)) >> 16) | (tv[i].y ^ thi) | (tv[i].w ^ thi);
          if (__builtin_amdgcn_ballot_w64(bad != 0u) == 0ull) break;
          __builtin_amdgcn_s_sleep(64);
        }
#pragma unroll
        for (int i = 0; i < 8; ++i) *(LAS unsigned*)(L + L_STAGE + (bt * 8 + i) * 256 + lane * 4) = (tv[i].x & 0xffffu) | (tv[i].z << 16);
      }
      wait_lgkm();
      lds_st(L, C_TABOK, 1u);
    }
    if (hw == 3) {
      lds_wait(L, C_WO + 3, 1u);
      const u64 T48 = ((u64)lds_ld(L, C_TLO) | ((u64)lds_ld(L, C_THI) << 32)) << 16;
      const GAS u64* fp = (const GAS u64*)(p.ws + WS_Q) + b * 4096 + ((lane & 7) * 32 + 4 * b + ((lane >> 3) & 3)) * 16;
      for (unsigned s = 0; s < SPIN_MAX; ++s) {
        const u64 f = __hip_atomic_load(fp, __ATOMIC_RELAXED, __HIP_MEMORY_SCOPE_AGENT);
        if (__builtin_amdgcn_ballot_w64(((f ^ T48) >> 16) != 0ull) == 0ull) break;
        __builtin_amdgcn_s_sleep(64);
      }
      asm volatile("" ::: "memory");
      lds_st(L, C_FLAGOK, 1u);
    }
    lds_wait(L, C_FLAGOK, 1u);
    if (hw == 0) STAMP(6);
    lds_wait(L, C_MERGE, X_H1START);
    {
      const int sg = lane & 3, jq = (lane >> 2) & 3;
      const unsigned rec = WS_REC + (unsigned)(grp * 4 + sg) * REC_BYTES;
      const unsigned doff = rec + hw * 512 + (32 * jq + 4 * g) * 4;
      v4u d0, d1; v2u hd; unsigned ok = 0u;
      for (unsigned s = 0; s < SPIN_MAX; ++s) {
        asm volatile("" ::: "memory");
        if ((ok & 1u) == 0u) {
          hd = __builtin_amdgcn_raw_buffer_load_b64(rws, (int)(rec + 2048 + hw * 8), 0, 16);
          d0 = ld_sc1(rws, doff); d1 = ld_sc1(rws, doff + 64);
          ok = hd.x & hd.y & d0.x & d0.y & d0.z & d0.w & d1.x & d1.y & d1.z & d1.w;
        }
        if (__builtin_amdgcn_ballot_w64((ok & 1u) == 0u) == 0ull) break;
      }
      if (hw == 0) STAMP(11);
      const float mh = __uint_as_float(hd.x), lh = __uint_as_float(hd.y);
      float mx = fmaxf(mh, dpp<DPP_XOR1>(mh)); mx = fmaxf(mx, dpp<DPP_XOR2>(mx));
      const float e = __builtin_amdgcn_exp2f((mh - mx) * LOG2E);
      float lt = e * lh; lt += dpp<DPP_XOR1>(lt); lt += dpp<DPP_XOR2>(lt);
      const float w = e * __builtin_amdgcn_rcpf(lt);
      float ov[8];
#pragma unroll
      for (int i = 0; i < 4; ++i) { ov[i] = __uint_as_float(d0[i]) * w; ov[4 + i] = __uint_as_float(d1[i]) * w; }
#pragma unroll
      for (int i = 0; i < 8; ++i) { ov[i] += dpp<DPP_XOR1>(ov[i]); ov[i] += dpp<DPP_XOR2>(ov[i]); }
      const v4u af = {pk_bf16(ov[0], ov[1]), pk_bf16(ov[2], ov[3]), pk_bf16(ov[4], ov[5]), pk_bf16(ov[6], ov[7])};
      f32x4 acc[8];
#pragma unroll
      for (int cc = 0; cc < 8; ++cc) acc[cc] = f32x4{0.f, 0.f, 0.f, 0.f};
#pragma unroll
      for (int j = 0; j < 4; ++j) {
        const v4u aj = jq == j ? af : v4u{0u, 0u, 0u, 0u};
#pragma unroll
        for (int cc = 0; cc < 8; ++cc) acc[cc] = mfma32(aj, v4u{wf[2 * j][cc].x, wf[2 * j][cc].y, wf[2 * j + 1][cc].x, wf[2 * j + 1][cc].y}, acc[cc]);
      }
      *(LAS float*)(L + L_Y + hw * 512 + lane * 4) = xrow_sum4(acc[0][0], acc[1][0], acc[2][0], acc[3][0]);
      *(LAS float*)(L + L_Y + hw * 512 + 256 + lane * 4) = xrow_sum4(acc[4][0], acc[5][0], acc[6][0], acc[7][0]);
    }
    wait_lgkm();
    unsigned old = 0;
    if (lane == 0) old = __hip_atomic_fetch_add((LAS unsigned*)(L + L_CTL + 4 * C_YCNT), 1u, __ATOMIC_RELAXED, __HIP_MEMORY_SCOPE_WORKGROUP);
    old = __builtin_amdgcn_readfirstlane(old);
    asm volatile("" ::: "memory");
    if (old == 3u) {
      STAMP(7);
      if (lane < 32) {
        const f32x4 y0 = *(LAS f32x4*)(L + L_Y + lane * 16), y1 = *(LAS f32x4*)(L + L_Y + 512 + lane * 16);
        const f32x4 y2 = *(LAS f32x4*)(L + L_Y + 1024 + lane * 16), y3 = *(LAS f32x4*)(L + L_Y + 1536 + lane * 16);
        const f32x4 y = (y0 + y1) + (y2 + y3);
        st_sc1(rws, yslot + lane * 16, v4u{tagf(y[0]), tagf(y[1]), tagf(y[2]), tagf(y[3])});
      }
    }
  }
}

}

extern "C" void kernel_launch(void* const* d_in, const int* in_sizes, int n_in, void* d_out, int out_size, void* d_ws, size_t ws_size,
                              hipStream_t stream) {
  Params p;
  p.x = (const unsigned short*)d_in[0]; p.k = (const unsigned short*)d_in[1]; p.v = (const unsigned short*)d_in[2];
  p.wq = (const unsigned short*)d_in[3]; p.wo = (const unsigned short*)d_in[4];
  p.w1 = (const unsigned short*)d_in[6]; p.b1 = (const unsigned short*)d_in[7]; p.w2 = (const unsigned short*)d_in[8]; p.b2 = (const unsigned short*)d_in[9];
  p.out = (unsigned short*)d_out; p.ws = (unsigned char*)d_ws;
  (void)hipFuncSetAttribute((const void*)fused_decode_kernel, hipFuncAttributeMaxDynamicSharedMemorySize, (int)L_END + 128);
  hipLaunchKernelGGL(fused_decode_kernel, dim3(NWG), dim3(NTHR), L_END + 128, stream, p);
}
```

```cpp
#include <hip/hip_runtime.h>
#include <hip/hip_bf16.h>
#include <stdint.h>

#define LAS __attribute__((address_space(3)))
#define GAS __attribute__((address_space(1)))
typedef unsigned v4u __attribute__((ext_vector_type(4)));
typedef unsigned v2u __attribute__((ext_vector_type(2)));
typedef int v4i __attribute__((ext_vector_type(4)));
typedef float f32x4 __attribute__((ext_vector_type(4)));
typedef float f32x2 __attribute__((ext_vector_type(2)));
typedef short s16x4 __attribute__((ext_vector_type(4)));
typedef __bf16 bf16x8 __attribute__((ext_vector_type(8)));
typedef __bf16 bf16x2 __attribute__((ext_vector_type(2)));
typedef unsigned long long u64;

#define STAMPS 0
#define TAB_EXP_LO 100u

namespace {

constexpr int NB = 8, DM = 512, HQ = 32, HKV = 8, DH = 128;
constexpr int NWG = 256, NTHR = 512;
constexpr int TILES = 32;
constexpr int NFIN = 4;
constexpr float LN_EPS = 1e-5f;
constexpr float SCALE = 0.08838834764831845f, LOG2E = 1.4426950408889634f;

constexpr unsigned WS_EPOCH = 0;
constexpr unsigned WS_Y = 8192;
constexpr unsigned WS_Q = WS_Y + NB * HKV * DM * 4;
constexpr unsigned REC_BYTES = 2304;
constexpr unsigned WS_REC = WS_Q + NB * 4096 * 8;
constexpr unsigned WS_TAB = WS_REC + NWG * REC_BYTES;
constexpr unsigned WS_STAMP = WS_TAB + 8192 * 8;
constexpr unsigned WS_END = WS_STAMP + NWG * 16 * 8;

constexpr unsigned L_RING = 0;
constexpr unsigned L_STAGE = 131072;
constexpr unsigned L_QBUF = L_STAGE + 16384;
constexpr unsigned L_PART = L_QBUF + 1024;
constexpr unsigned L_XN = L_PART, XN_ROW = 1040;
constexpr unsigned L_ML = L_PART + 8320;
constexpr unsigned L_Y = L_ML + 128;
constexpr unsigned L_T = L_Y + 2048;
constexpr unsigned L_MLP = L_T + 128;
constexpr unsigned L_CTL = L_MLP + 512;
constexpr unsigned L_END = L_CTL + 128;
static_assert(L_END + 128 <= 160 * 1024, "LDS budget");
enum { C_QRDY = 0, C_XN = 1, C_MERGE = 2, C_YCNT = 3, C_TLO = 4, C_THI = 5, C_TRDY = 6, C_NEAR = 7, C_FLAGOK = 8, C_TCNT = 9, C_TABOK = 10, C_WO = 12   };

constexpr unsigned SPIN_MAX = 1u << 18;

struct Params {
  const unsigned short* k; const unsigned short* v; const unsigned short* wo; unsigned char* ws;
  const unsigned short* x; const unsigned short* wq;
  const unsigned short* w1; const unsigned short* b1; const unsigned short* w2; const unsigned short* b2;
  unsigned short* out;
};

__device__ __forceinline__ v4i mkrsrc(const void* p, unsigned bytes) {
  const u64 a = (u64)p;
  v4i r; r.x = __builtin_amdgcn_readfirstlane((int)(unsigned)a); r.y = __builtin_amdgcn_readfirstlane((int)((unsigned)(a >> 32) & 0xffffu));
  r.z = (int)bytes; r.w = 0x00020000; return r;
}
__device__ __forceinline__ void dma4_nt(v4i rs, unsigned v0, unsigned v1, unsigned v2, unsigned v3, unsigned soff, unsigned lds) {
  asm volatile("s_mov_b32 m0, %6\n\ts_nop 0\n\t"
               "buffer_load_dwordx4 %0, %4, %5 offen nt lds\n\t"
               "buffer_load_dwordx4 %1, %4, %5 offen offset:1024 nt lds\n\t"
               "buffer_load_dwordx4 %2, %4, %5 offen offset:2048 nt lds\n\t"
               "buffer_load_dwordx4 %3, %4, %5 offen offset:3072 nt lds"
               :: "v"(v0), "v"(v1), "v"(v2), "v"(v3), "s"(rs), "s"(soff), "s"(lds) : "memory");
}
__device__ __forceinline__ void dma4_w(v4i rs, unsigned v0, unsigned v1, unsigned v2, unsigned v3, unsigned soff, unsigned lds) {
  asm volatile("s_mov_b32 m0, %6\n\ts_nop 0\n\t"
               "buffer_load_dwordx4 %0, %4, %5 offen lds\n\t"
               "buffer_load_dwordx4 %1, %4, %5 offen offset:1024 lds\n\t"
               "buffer_load_dwordx4 %2, %4, %5 offen offset:2048 lds\n\t"
               "buffer_load_dwordx4 %3, %4, %5 offen offset:3072 lds"
               :: "v"(v0), "v"(v1), "v"(v2), "v"(v3), "s"(rs), "s"(soff), "s"(lds) : "memory");
}
template <int N> __device__ __forceinline__ void wait_vm() { asm volatile("s_waitcnt vmcnt(%0)" :: "i"(N) : "memory"); }
__device__ __forceinline__ void wait_lgkm() { asm volatile("s_waitcnt lgkmcnt(0)" ::: "memory"); }
__device__ __forceinline__ void wg_barrier() { asm volatile("s_waitcnt lgkmcnt(0)\n\ts_barrier" ::: "memory"); }

template <int CTRL> __device__ __forceinline__ float dpp(float x) {
  return __builtin_bit_cast(float, __builtin_amdgcn_mov_dpp(__builtin_bit_cast(int, x), CTRL, 0xf, 0xf, true));
}
constexpr int DPP_XOR1 = 0xB1, DPP_XOR2 = 0x4E, DPP_HMIRROR = 0x141, DPP_ROR8 = 0x128;
__device__ __forceinline__ float xrow_max(float x) {
  auto s = __builtin_amdgcn_permlane16_swap(__float_as_uint(x), __float_as_uint(x), false, false);
  x = fmaxf(__uint_as_float(s[0]), __uint_as_float(s[1]));
  auto t = __builtin_amdgcn_permlane32_swap(__float_as_uint(x), __float_as_uint(x), false, false);
  return fmaxf(__uint_as_float(t[0]), __uint_as_float(t[1]));
}
__device__ __forceinline__ float xrow_sum(float x) {
  auto s = __builtin_amdgcn_permlane16_swap(__float_as_uint(x), __float_as_uint(x), false, false);
  x = __uint_as_float(s[0]) + __uint_as_float(s[1]);
  auto t = __builtin_amdgcn_permlane32_swap(__float_as_uint(x), __float_as_uint(x), false, false);
  return __uint_as_float(t[0]) + __uint_as_float(t[1]);
}
__device__ __forceinline__ float row16_sum(float x) {
  x += dpp<DPP_XOR1>(x); x += dpp<DPP_XOR2>(x); x += dpp<DPP_HMIRROR>(x); x += dpp<DPP_ROR8>(x); return x;
}
__device__ __forceinline__ float wave_sum(float x) { return xrow_sum(row16_sum(x)); }
__device__ __forceinline__ float xrow_sum4(float a, float b, float c, float d) {
  auto s01 = __builtin_amdgcn_permlane16_swap(__float_as_uint(a), __float_as_uint(b), false, false);
  auto s23 = __builtin_amdgcn_permlane16_swap(__float_as_uint(c), __float_as_uint(d), false, false);
  const float x = __uint_as_float(s01[0]) + __uint_as_float(s01[1]), y = __uint_as_float(s23[0]) + __uint_as_float(s23[1]);
  auto t = __builtin_amdgcn_permlane32_swap(__float_as_uint(x), __float_as_uint(y), false, false);
  return __uint_as_float(t[0]) + __uint_as_float(t[1]);
}
__device__ __forceinline__ unsigned pk_bf16(float lo, float hi) {
  bf16x2 r; r[0] = (__bf16)lo; r[1] = (__bf16)hi; return __builtin_bit_cast(unsigned, r);
}
__device__ __forceinline__ float bf_lo(unsigned u) { return __uint_as_float(u << 16); }
__device__ __forceinline__ float bf_hi(unsigned u) { return __uint_as_float(u & 0xffff0000u); }
__device__ __forceinline__ float bf1(unsigned short u) { return __uint_as_float((unsigned)u << 16); }
__device__ __forceinline__ float rbf(float x) { return bf_lo(pk_bf16(x, x)); }
__device__ __forceinline__ f32x4 mfma32(v4u a, v4u b, f32x4 c) {
  return __builtin_amdgcn_mfma_f32_16x16x32_bf16(__builtin_bit_cast(bf16x8, a), __builtin_bit_cast(bf16x8, b), c, 0, 0, 0);
}
__device__ __forceinline__ f32x4 mfma16(v2u a, v2u b, f32x4 c) {
  return __builtin_amdgcn_mfma_f32_16x16x16bf16_1k(__builtin_bit_cast(s16x4, a), __builtin_bit_cast(s16x4, b), c, 0, 0, 0);
}
__device__ __forceinline__ v2u tr_read(LAS unsigned char* p) {
  return __builtin_bit_cast(v2u, __builtin_amdgcn_ds_read_tr16_b64_v4i16((LAS s16x4*)p));
}
__device__ __forceinline__ v4u ld_sc1(__amdgpu_buffer_rsrc_t rs, unsigned off) { return __builtin_amdgcn_raw_buffer_load_b128(rs, (int)off, 0, 16); }
__device__ __forceinline__ void st_sc1(__amdgpu_buffer_rsrc_t rs, unsigned off, v4u v) { __builtin_amdgcn_raw_buffer_store_b128(v, rs, (int)off, 0, 16); }
__device__ __forceinline__ unsigned lds_ld(LAS unsigned char* L, int idx) { return *(volatile LAS unsigned*)(L + L_CTL + 4 * idx); }
__device__ __forceinline__ void lds_st(LAS unsigned char* L, int idx, unsigned v) { *(volatile LAS unsigned*)(L + L_CTL + 4 * idx) = v; }
__device__ __forceinline__ void lds_wait(LAS unsigned char* L, int idx, unsigned want) {
  for (unsigned s = 0; s < SPIN_MAX; ++s) { if (lds_ld(L, idx) >= want) break; __builtin_amdgcn_s_sleep(1); }
  asm volatile("" ::: "memory");
}
__device__ __forceinline__ unsigned tagf(float f) { return __float_as_uint(f) | 1u; }
#define STAMP(i) do { } while (0)

struct AttnState { float m, l; f32x4 o[8]; };

constexpr int slot_of(int n) { return n < 24 ? (n & 7) : (n & 3); }
struct StreamCtx {
  LAS unsigned char* L; unsigned ring; v4i rk, rv; unsigned vk[4], vv[2], ka[4], va[8]; unsigned tbase;
  template <int M> __device__ __forceinline__ void issue(int j) const {
    if constexpr ((M & 1) == 0) dma4_nt(rk, vk[0], vk[1], vk[2], vk[3], tbase + (unsigned)j * 65536u, ring + slot_of(M) * 4096);
    else dma4_nt(rv, vv[0], vv[1], vv[0], vv[1], tbase + (unsigned)j * 65536u, ring + slot_of(M) * 4096);
  }
  template <int N> __device__ __forceinline__ void step(int t) const {
    wait_vm<(N + 1 < 2 * TILES) ? 4 : 0>();
    if constexpr (N + 2 < 2 * TILES) issue<N + 2>(t + 1);
  }
};

__device__ __forceinline__ void softmax_pv(AttnState& st, const f32x4 sc, const v2u (&vf)[8], bool lds_pending) {
  const unsigned r01 = pk_bf16(sc[0], sc[1]), r23 = pk_bf16(sc[2], sc[3]);
  const unsigned u01 = pk_bf16(bf_lo(r01) * SCALE, bf_hi(r01) * SCALE), u23 = pk_bf16(bf_lo(r23) * SCALE, bf_hi(r23) * SCALE);
  const float s0 = bf_lo(u01), s1 = bf_hi(u01), s2 = bf_lo(u23), s3 = bf_hi(u23);
  const float tm = xrow_max(fmaxf(fmaxf(s0, s1), fmaxf(s2, s3)));
  if (__builtin_amdgcn_ballot_w64(tm > st.m) != 0ull) {
    const float mn = fmaxf(st.m, tm);
    const float al = __builtin_amdgcn_exp2f((st.m - mn) * LOG2E);
    st.m = mn; st.l *= al;
    const float a0 = dpp<0x00>(al), a1 = dpp<0x55>(al), a2 = dpp<0xAA>(al), a3 = dpp<0xFF>(al);
#pragma unroll
    for (int c = 0; c < 8; ++c) { st.o[c][0] *= a0; st.o[c][1] *= a1; st.o[c][2] *= a2; st.o[c][3] *= a3; }
  }
  const float p0 = __builtin_amdgcn_exp2f((s0 - st.m) * LOG2E), p1 = __builtin_amdgcn_exp2f((s1 - st.m) * LOG2E);
  const float p2 = __builtin_amdgcn_exp2f((s2 - st.m) * LOG2E), p3 = __builtin_amdgcn_exp2f((s3 - st.m) * LOG2E);
  st.l += (p0 + p1) + (p2 + p3);
  v2u pf; pf.x = pk_bf16(p0, p1); pf.y = pk_bf16(p2, p3);
  if (lds_pending) wait_lgkm();
#pragma unroll
  for (int c = 0; c < 8; ++c) st.o[c] = mfma16(pf, vf[c], st.o[c]);
}
struct TileRegs { v4u kf[4]; v2u vf[8]; };
template <int TC> __device__ __forceinline__ void read_k(TileRegs& tr, const StreamCtx& cx) {
#pragma unroll
  for (int s = 0; s < 4; ++s) tr.kf[s] = *(LAS v4u*)(cx.L + cx.ring + slot_of(2 * TC) * 4096 + cx.ka[s]);
}
template <int TC> __device__ __forceinline__ void read_v(TileRegs& tr, const StreamCtx& cx) {
#pragma unroll
  for (int c = 0; c < 8; ++c) tr.vf[c] = tr_read(cx.L + cx.ring + slot_of(2 * TC) * 4096 + 4096 + cx.va[c]);
}
__device__ __forceinline__ f32x4 qk(const TileRegs& tr, const v4u (&qf)[4]) {
  f32x4 sc = {0.f, 0.f, 0.f, 0.f};
#pragma unroll
  for (int s = 0; s < 4; ++s) sc = mfma32(tr.kf[s], qf[s], sc);
  return sc;
}
template <int TC> __device__ __forceinline__ void attn_tile(AttnState& st, const StreamCtx& cx, const v4u (&qf)[4], int t) {
  TileRegs tr;
  cx.step<2 * TC>(t); read_k<TC>(tr, cx);
  const f32x4 sc = qk(tr, qf);
  cx.step<2 * TC + 1>(t); read_v<TC>(tr, cx);
  softmax_pv(st, sc, tr.vf, true);
}
template <int TC> __device__ __forceinline__ void ring_tile(AttnState& st, const StreamCtx& cx, const v4u (&qf)[4]) {
  TileRegs tr; read_k<TC>(tr, cx); read_v<TC>(tr, cx);
  softmax_pv(st, qk(tr, qf), tr.vf, true);
}
template <int TC> __device__ __forceinline__ void park_tile(TileRegs& tr, const StreamCtx& cx) {
  cx.step<2 * TC>(TC); read_k<TC>(tr, cx);
  cx.step<2 * TC + 1>(TC); read_v<TC>(tr, cx);
  wait_lgkm();
}
__device__ __forceinline__ void parked_tile(AttnState& st, const TileRegs& tr, const v4u (&qf)[4]) { softmax_pv(st, qk(tr, qf), tr.vf, false); }
__device__ __forceinline__ f32x2 rbf2(f32x2 v) { const unsigned u = pk_bf16(v.x, v.y); return f32x2{bf_lo(u), bf_hi(u)}; }
__device__ __forceinline__ void ln_stats(const f32x2 (&z)[4], float& mb, float& rs) {
  const float pv = __builtin_bit_cast(float, __builtin_amdgcn_readfirstlane(__builtin_bit_cast(int, z[0].x)));
  f32x2 s = {0.f, 0.f}, q = {0.f, 0.f};
#pragma unroll
  for (int e = 0; e < 4; ++e) { const f32x2 d = z[e] - f32x2{pv, pv}; s += d; q = __builtin_elementwise_fma(d, d, q); }
  float s1 = s.x + s.y, q1 = q.x + q.y;
  s1 += dpp<DPP_XOR1>(s1); q1 += dpp<DPP_XOR1>(q1); s1 += dpp<DPP_XOR2>(s1); q1 += dpp<DPP_XOR2>(q1);
  s1 += dpp<DPP_HMIRROR>(s1); q1 += dpp<DPP_HMIRROR>(q1); s1 += dpp<DPP_ROR8>(s1); q1 += dpp<DPP_ROR8>(q1);
  const float dm = xrow_sum(s1) * (1.f / DM), q2 = xrow_sum(q1) * (1.f / DM);
  const float var = fmaxf(fmaf(-dm, dm, q2), 0.f);
  mb = rbf(pv + dm);
  rs = __builtin_amdgcn_rcpf(rbf(__builtin_amdgcn_sqrtf(rbf(rbf(var) + LN_EPS))));
}
__device__ __forceinline__ f32x2 ln_apply(f32x2 z, float mb, float rs) { return rbf2(rbf2(z - f32x2{mb, mb}) * f32x2{rs, rs}); }

constexpr float GELU_K0 = -2.f * 0.7978845608028654f * LOG2E, GELU_K1 = GELU_K0 * 0.044715f;
struct MlpCoef { float k3, k2, k1, k0, wa, wb; };
__device__ __forceinline__ MlpCoef mlp_coef(unsigned short w1r, unsigned short b1r, unsigned short w2r) {
  constexpr float K0 = GELU_K0, K1 = GELU_K1;
  const float a = bf1(w1r), bb = bf1(b1r), w2 = bf1(w2r);
  MlpCoef c;
  c.k3 = K1 * a * a * a; c.k2 = 3.f * K1 * a * a * bb; c.k1 = a * fmaf(3.f * K1 * bb, bb, K0); c.k0 = bb * fmaf(K1 * bb, bb, K0);
  c.wa = w2 * a; c.wb = w2 * bb;
  return c;
}
__device__ __forceinline__ f32x2 mlp_term(const MlpCoef& c, f32x2 t, f32x2 acc) {
  f32x2 ar = __builtin_elementwise_fma(f32x2{c.k3, c.k3}, t, f32x2{c.k2, c.k2});
  ar = __builtin_elementwise_fma(ar, t, f32x2{c.k1, c.k1});
  ar = __builtin_elementwise_fma(ar, t, f32x2{c.k0, c.k0});
  const f32x2 wu = __builtin_elementwise_fma(f32x2{c.wa, c.wa}, t, f32x2{c.wb, c.wb});
  f32x2 d = {__builtin_amdgcn_exp2f(ar.x), __builtin_amdgcn_exp2f(ar.y)};
  d += f32x2{1.f, 1.f};
  const f32x2 r = {__builtin_amdgcn_rcpf(d.x), __builtin_amdgcn_rcpf(d.y)};
  return __builtin_elementwise_fma(wu, r, acc);
}
constexpr unsigned TAB_LO = TAB_EXP_LO << 7, TAB_N = 4096u, TAB_ZERO = TAB_N - 1u;

__global__ void __launch_bounds__(NTHR, 2) fused_decode_kernel(Params p) {
  extern __shared__ __attribute__((aligned(16))) unsigned char lds_raw[];
  LAS unsigned char* L = (LAS unsigned char*)lds_raw;
  const int tid = threadIdx.x, lane = tid & 63, wave = __builtin_amdgcn_readfirstlane(tid >> 6);
  const int c = blockIdx.x;
  const int b = c >> 5, kvh = c & 7, sp = (c >> 3) & 3, grp = b * 8 + kvh;
  const int rid = grp * 4 + sp;
  const int cq = (c & 7) * 32 + (c >> 3);
  const int e16 = c & 31;
  const int g = lane >> 4, c16 = lane & 15;
  const __amdgpu_buffer_rsrc_t rws = __builtin_amdgcn_make_buffer_rsrc(p.ws, 0, WS_END, 0x00020000);

  const unsigned ra = 256 * (4 * g + (c16 >> 2)) + 16 * (((lane >> 1) & 1) ^ (((4 * g + (c16 >> 2)) & 7) << 1)) + 8 * (lane & 1);

  if (wave < 4) {
    if (wave == 0) STAMP(0);
    __builtin_amdgcn_s_setprio(2);
    StreamCtx cx;
    cx.L = L; cx.rk = mkrsrc(p.k, 1u << 27); cx.rv = mkrsrc(p.v, 1u << 27);
#pragma unroll
    for (int i = 0; i < 4; ++i) cx.vk[i] = g * 256 + 16 * (c16 ^ (4 * i + g));
#pragma unroll
    for (int i = 0; i < 2; ++i) cx.vv[i] = g * 256 + 16 * (c16 ^ (((4 * i + g) & 7) << 1));
    cx.ring = L_RING + wave * 32768;
    cx.tbase = (unsigned)grp * (2048u * 1024u) + (unsigned)(sp * 4 + wave) * 4096u;
    cx.issue<0>(0); cx.issue<1>(0);
    wg_barrier();
#pragma unroll
    for (int s = 0; s < 4; ++s) cx.ka[s] = 256 * c16 + 16 * ((4 * s + g) ^ c16);
#pragma unroll
    for (int cc = 0; cc < 8; ++cc) cx.va[cc] = ra ^ (unsigned)(cc << 5);
    TileRegs pk0, pk1, pk2, pk3;
    park_tile<0>(pk0, cx); park_tile<1>(pk1, cx); park_tile<2>(pk2, cx); park_tile<3>(pk3, cx);
    cx.step<8>(4); cx.step<9>(4); cx.step<10>(5);
    if (wave == 0) STAMP(12);
    lds_wait(L, C_QRDY, 1u);
    v4u qf[4];
#pragma unroll
    for (int s = 0; s < 4; ++s) qf[s] = *(LAS v4u*)(L + L_QBUF + (lane & 3) * 256 + s * 64 + g * 16);
    AttnState st; st.m = -INFINITY; st.l = 0.f;
#pragma unroll
    for (int cc = 0; cc < 8; ++cc) st.o[cc] = f32x4{0.f, 0.f, 0.f, 0.f};
    parked_tile(st, pk0, qf);
    cx.step<11>(5); parked_tile(st, pk1, qf);
    cx.step<12>(6); parked_tile(st, pk2, qf);
    cx.step<13>(6); parked_tile(st, pk3, qf);
    ring_tile<4>(st, cx, qf); cx.step<14>(7);
    cx.step<15>(7); ring_tile<5>(st, cx, qf);
    cx.step<16>(8); ring_tile<6>(st, cx, qf);
    cx.step<17>(8); ring_tile<7>(st, cx, qf);
    cx.step<18>(9); ring_tile<8>(st, cx, qf);
    cx.step<19>(9); ring_tile<9>(st, cx, qf);
    attn_tile<10>(st, cx, qf, 10); attn_tile<11>(st, cx, qf, 11);
    lds_st(L, C_WO + wave, 1u);
    if (wave == 0) STAMP(13);
    for (int j = 12; j < TILES - 2; j += 2) { attn_tile<12>(st, cx, qf, j); attn_tile<13>(st, cx, qf, j + 1); }
    attn_tile<30>(st, cx, qf, 30); attn_tile<31>(st, cx, qf, 31);
    if (wave == 0) STAMP(2);
    const float lsum = xrow_sum(st.l);
#pragma unroll
    for (int cc = 0; cc < 8; ++cc) {
      const float v = g == 0 ? st.o[cc][0] : g == 1 ? st.o[cc][1] : g == 2 ? st.o[cc][2] : st.o[cc][3];
      *(LAS float*)(L + L_PART + wave * 2048 + g * 512 + (16 * cc + c16) * 4) = v;
    }
    if (lane < 4) *(LAS v2u*)(L + L_ML + wave * 32 + lane * 8) = v2u{__float_as_uint(st.m), __float_as_uint(lsum)};
    wait_lgkm();
    unsigned old = 0;
    if (lane == 0) old = __hip_atomic_fetch_add((LAS unsigned*)(L + L_CTL + 4 * C_MERGE), 1u, __ATOMIC_RELAXED, __HIP_MEMORY_SCOPE_WORKGROUP);
    old = __builtin_amdgcn_readfirstlane(old);
    asm volatile("" ::: "memory");
    if (old == 3u) {
      float mw[4], lw[4];
#pragma unroll
      for (int w = 0; w < 4; ++w) { const v2u ml = *(LAS v2u*)(L + L_ML + w * 32 + g * 8); mw[w] = __uint_as_float(ml.x); lw[w] = __uint_as_float(ml.y); }
      const float M = fmaxf(fmaxf(mw[0], mw[1]), fmaxf(mw[2], mw[3]));
      float Ls = 0.f; f32x4 a0 = {0.f, 0.f, 0.f, 0.f}, a1 = {0.f, 0.f, 0.f, 0.f};
#pragma unroll
      for (int w = 0; w < 4; ++w) {
        const float f = __builtin_amdgcn_exp2f((mw[w] - M) * LOG2E);
        const f32x4 x0 = *(LAS f32x4*)(L + L_PART + w * 2048 + g * 512 + c16 * 16), x1 = *(LAS f32x4*)(L + L_PART + w * 2048 + g * 512 + 256 + c16 * 16);
        Ls = fmaf(f, lw[w], Ls); a0 += f * x0; a1 += f * x1;
      }
      const unsigned rec = WS_REC + (unsigned)rid * REC_BYTES;
      st_sc1(rws, rec + g * 512 + c16 * 16, v4u{tagf(a0[0]), tagf(a0[1]), tagf(a0[2]), tagf(a0[3])});
      st_sc1(rws, rec + g * 512 + 256 + c16 * 16, v4u{tagf(a1[0]), tagf(a1[1]), tagf(a1[2]), tagf(a1[3])});
      if (c16 == 0) __builtin_amdgcn_raw_buffer_store_b64(v2u{tagf(M), tagf(Ls)}, rws, (int)(rec + 2048 + g * 8), 0, 16);
      STAMP(3);
    }
    if (wave == 0 && (e16 % (32 / NFIN)) == 0) {
      const unsigned short b2r = p.b2[0];
      const v2u xr0 = *(const GAS v2u*)(p.x + b * DM + lane * 4), xr1 = *(const GAS v2u*)(p.x + b * DM + 256 + lane * 4);
      lds_wait(L, C_FLAGOK, 1u);
      lds_wait(L, C_TABOK, 1u);
      const unsigned tl = lds_ld(L, C_TLO), th = lds_ld(L, C_THI);
      const unsigned yoff = WS_Y + (unsigned)b * 16384u + lane * 16;
      v4u yv[16]; unsigned pend = 0xffffu;
      for (unsigned s = 0; s < SPIN_MAX; ++s) {
        asm volatile("" ::: "memory");
#pragma unroll
        for (int i = 0; i < 16; ++i) if ((pend >> i) & 1u) yv[i] = ld_sc1(rws, yoff + i * 1024);
#pragma unroll
        for (int i = 0; i < 16; ++i) if ((pend >> i) & 1u) {
          const unsigned ok = yv[i].x & yv[i].y & yv[i].z & yv[i].w;
          if (__builtin_amdgcn_ballot_w64((ok & 1u) == 0u) == 0ull) pend &= ~(1u << i);
        }
        if (pend == 0u) break;
        if (__builtin_popcount(pend) > 4) __builtin_amdgcn_s_sleep(1);
      }
      STAMP(14);
      f32x2 hv[4], z[4];
#pragma unroll
      for (int hf = 0; hf < 2; ++hf) {
        f32x4 ys[8];
#pragma unroll
        for (int kk = 0; kk < 8; ++kk) ys[kk] = __builtin_bit_cast(f32x4, yv[2 * kk + hf]);
        const f32x4 y = ((ys[0] + ys[1]) + (ys[2] + ys[3])) + ((ys[4] + ys[5]) + (ys[6] + ys[7]));
        const v2u xr = hf ? xr1 : xr0;
        hv[2 * hf] = rbf2(f32x2{bf_lo(xr.x), bf_hi(xr.x)} + rbf2(f32x2{y[0], y[1]}));
        hv[2 * hf + 1] = rbf2(f32x2{bf_lo(xr.y), bf_hi(xr.y)} + rbf2(f32x2{y[2], y[3]}));
      }
      float mb, rs;
      ln_stats(hv, mb, rs);
#pragma unroll
      for (int e = 0; e < 4; ++e) z[e] = ln_apply(hv[e], mb, rs);
      ln_stats(z, mb, rs);
      STAMP(8);
      const float b2f = bf1(b2r);
#pragma unroll
      for (int hf = 0; hf < 2; ++hf) {
        if (NFIN > 1 && (e16 >> 4) != hf) continue;
        constexpr int NL = NFIN >= 2 ? 128 / NFIN : 64;
        const int l0 = NFIN >= 2 ? (4 * e16) & 63 : 0;
        const bool mine = lane >= l0 && lane < l0 + NL;
        const f32x2 za = ln_apply(z[2 * hf], mb, rs), zb = ln_apply(z[2 * hf + 1], mb, rs);
        const float tz[4] = {za.x, za.y, zb.x, zb.y}, h4[4] = {hv[2 * hf].x, hv[2 * hf].y, hv[2 * hf + 1].x, hv[2 * hf + 1].y};
        unsigned idx[4]; bool oot[4];
#pragma unroll
        for (int e = 0; e < 4; ++e) {
          const unsigned bits = __float_as_uint(tz[e]) >> 16, mag = bits & 0x7fffu;
          const unsigned rel = mag == 0u ? TAB_ZERO : mag - TAB_LO;
          const bool outside = mag != 0u && rel >= TAB_ZERO;
          oot[e] = mine && outside;
          idx[e] = outside ? 0u : rel * 2u + (bits >> 15);
        }
        float y0 = bf1(*(LAS unsigned short*)(L + L_STAGE + idx[0] * 2u)), y1 = bf1(*(LAS unsigned short*)(L + L_STAGE + idx[1] * 2u));
        float y2 = bf1(*(LAS unsigned short*)(L + L_STAGE + idx[2] * 2u)), y3 = bf1(*(LAS unsigned short*)(L + L_STAGE + idx[3] * 2u));
        if (__builtin_amdgcn_ballot_w64(oot[0] || oot[1] || oot[2] || oot[3]) != 0ull) {
#pragma nounroll
          for (int e = 0; e < 4; ++e) {
            const float te = e == 0 ? tz[0] : e == 1 ? tz[1] : e == 2 ? tz[2] : tz[3];
            u64 todo = __builtin_amdgcn_ballot_w64(e == 0 ? oot[0] : e == 1 ? oot[1] : e == 2 ? oot[2] : oot[3]);
            while (todo != 0ull) {
              const int src = __builtin_ctzll(todo); todo &= todo - 1ull;
              const float tv = __builtin_bit_cast(float, __builtin_amdgcn_readlane(__builtin_bit_cast(int, te), src));
              float acc = 0.f;
#pragma nounroll
              for (int i = 0; i < 28; ++i) {
                const float u = fmaf(tv, bf1(p.w1[lane + 64 * i]), bf1(p.b1[lane + 64 * i]));
                const float ex = __builtin_amdgcn_exp2f(u * fmaf(u * u, GELU_K1, GELU_K0));
                acc = fmaf(u * bf1(p.w2[lane + 64 * i]), __builtin_amdgcn_rcpf(1.f + ex), acc);
              }
              const float yd = rbf(wave_sum(acc));
              if (lane == src) { if (e == 0) y0 = yd; else if (e == 1) y1 = yd; else if (e == 2) y2 = yd; else y3 = yd; }
            }
          }
        }
        if (mine) {
          const v2u o = {pk_bf16(h4[0] + rbf(y0 + b2f), h4[1] + rbf(y1 + b2f)), pk_bf16(h4[2] + rbf(y2 + b2f), h4[3] + rbf(y3 + b2f))};
          *(GAS v2u*)(p.out + b * DM + hf * 256 + lane * 4) = o;
        }
      }
      if (c == 0 && lane == 0) __hip_atomic_store((GAS u64*)(p.ws + WS_EPOCH), (u64)tl | ((u64)th << 32), __ATOMIC_RELAXED, __HIP_MEMORY_SCOPE_AGENT);
      STAMP(9);
    }
  } else {
    const int hw = wave - 4;
    const unsigned yslot = WS_Y + (((unsigned)b * 8u + (unsigned)kvh) * 512u + (unsigned)sp * 128u) * 4u;
    if (hw == 0) {
      const v4i rq = mkrsrc(p.wq, DM * HQ * DH * 2);
      const unsigned vo = (lane >> 1) * 8192 + (lane & 1) * 16;
#pragma unroll
      for (int s4 = 0; s4 < 4; ++s4)
        dma4_w(rq, vo, vo + 32u * 8192u - 1024u, vo + 64u * 8192u - 2048u, vo + 96u * 8192u - 3072u, (unsigned)cq * 32u + (unsigned)s4 * (128u * 8192u), L_STAGE + s4 * 4096);
      const u64 ep = __hip_atomic_load((GAS u64*)(p.ws + WS_EPOCH), __ATOMIC_RELAXED, __HIP_MEMORY_SCOPE_AGENT);
      wg_barrier();
      const u64 T = ep + 0x9E3779B97F4A7C15ull;
      if (lane == 0) { lds_st(L, C_TLO, (unsigned)T); lds_st(L, C_THI, (unsigned)(T >> 32)); }
      wait_vm<0>();
      STAMP(1);
      lds_wait(L, C_XN, 3u);
      f32x4 d = {0.f, 0.f, 0.f, 0.f};
      const unsigned ta = (8 * g + (c16 >> 2)) * 32 + (lane & 3) * 8;
#pragma unroll
      for (int s = 0; s < 16; ++s) {
        const v2u lo = tr_read(L + L_STAGE + s * 1024 + ta), hi = tr_read(L + L_STAGE + s * 1024 + ta + 128);
        const v4u xf = *(LAS v4u*)(L + L_XN + (c16 & 7) * XN_ROW + s * 64 + g * 16);
        d = mfma32(v4u{lo.x, lo.y, hi.x, hi.y}, xf, d);
      }
      const unsigned p0 = pk_bf16(d[0], d[1]), p1 = pk_bf16(d[2], d[3]);
      const int src = (c16 + 16 * (2 * (c16 >> 3) + (g >> 1))) * 4;
      const unsigned q0 = (unsigned)__builtin_amdgcn_ds_bpermute(src, (int)p0), q1 = (unsigned)__builtin_amdgcn_ds_bpermute(src, (int)p1);
      const unsigned qp = (g & 1) ? q1 : q0;
      const unsigned tlo = (unsigned)T << 16, thi = (unsigned)(T >> 16);
      st_sc1(rws, WS_Q + ((unsigned)(c16 & 7) * 4096u + (unsigned)cq * 16u) * 8u + (4 * (c16 >> 3) + g) * 16u,
             v4u{(qp & 0xffffu) | tlo, thi, (qp >> 16) | tlo, thi});
      STAMP(4);
      const unsigned qoff = WS_Q + ((unsigned)b * 4096u + (unsigned)kvh * 512u) * 8u + lane * 16u;
      v4u a0, a1, a2, a3; unsigned bad = 1u;
      __builtin_amdgcn_s_sleep(64);
      for (unsigned s = 0; s < SPIN_MAX; ++s) {
        asm volatile("" ::: "memory");
        if (bad != 0u) {
          a0 = ld_sc1(rws, qoff); a1 = ld_sc1(rws, qoff + 1024); a2 = ld_sc1(rws, qoff + 2048); a3 = ld_sc1(rws, qoff + 3072);
          bad = (((a0.x ^ tlo) | (a0.z ^ tlo) | (a1.x ^ tlo) | (a1.z ^ tlo) | (a2.x ^ tlo) | (a2.z ^ tlo) | (a3.x ^ tlo) | (a3.z ^ tlo)) >> 16) |
                (a0.y ^ thi) | (a0.w ^ thi) | (a1.y ^ thi) | (a1.w ^ thi) | (a2.y ^ thi) | (a2.w ^ thi) | (a3.y ^ thi) | (a3.w ^ thi);
        }
        if (__builtin_amdgcn_ballot_w64(bad != 0u) == 0ull) break;
        __builtin_amdgcn_s_sleep(32);
      }
      *(LAS unsigned*)(L + L_QBUF + lane * 4) = (a0.x & 0xffffu) | (a0.z << 16);
      *(LAS unsigned*)(L + L_QBUF + 256 + lane * 4) = (a1.x & 0xffffu) | (a1.z << 16);
      *(LAS unsigned*)(L + L_QBUF + 512 + lane * 4) = (a2.x & 0xffffu) | (a2.z << 16);
      *(LAS unsigned*)(L + L_QBUF + 768 + lane * 4) = (a3.x & 0xffffu) | (a3.z << 16);
      wait_lgkm();
      lds_st(L, C_QRDY, 1u);
      STAMP(5);
    } else if (hw != 3) {
      v4u xv[4];
#pragma unroll
      for (int i = 0; i < 4; ++i) xv[i] = *((const GAS v4u*)(p.x + ((hw - 1) * 4 + i) * DM) + lane);
      wg_barrier();
#pragma unroll
      for (int i = 0; i < 4; ++i) {
        f32x2 z[4]; float mb, rs; v4u o;
#pragma unroll
        for (int e = 0; e < 4; ++e) z[e] = f32x2{bf_lo(xv[i][e]), bf_hi(xv[i][e])};
        ln_stats(z, mb, rs);
#pragma unroll
        for (int e = 0; e < 4; ++e) { const f32x2 t = rbf2(z[e] - f32x2{mb, mb}) * f32x2{rs, rs}; o[e] = pk_bf16(t.x, t.y); }
        *(LAS v4u*)(L + L_XN + ((hw - 1) * 4 + i) * XN_ROW + lane * 16) = o;
      }
      wait_lgkm();
      if (lane == 0) __hip_atomic_fetch_add((LAS unsigned*)(L + L_CTL + 4 * C_XN), 1u, __ATOMIC_RELAXED, __HIP_MEMORY_SCOPE_WORKGROUP);
    } else {
      const v4u z = {0u, 0u, 0u, 0u};
      const unsigned rec = WS_REC + (unsigned)rid * REC_BYTES;
      st_sc1(rws, rec + lane * 16, z); st_sc1(rws, rec + 1024 + lane * 16, z);
      if (lane < 2) st_sc1(rws, rec + 2048 + lane * 16, z);
      if (lane < 32) st_sc1(rws, yslot + lane * 16, z);
      if (lane < 32) lds_st(L, lane, 0u);
      wg_barrier();
      wait_vm<0>();
      if (lane == 0) __hip_atomic_fetch_add((LAS unsigned*)(L + L_CTL + 4 * C_XN), 1u, __ATOMIC_RELAXED, __HIP_MEMORY_SCOPE_WORKGROUP);
    }
    {
      MlpCoef cf[7];
      const int f0 = tid & 255;
#pragma unroll
      for (int i = 0; i < 7; ++i) cf[i] = mlp_coef(p.w1[f0 + 256 * i], p.b1[f0 + 256 * i], p.w2[f0 + 256 * i]);
#pragma nounroll
      for (int ch = 0; ch < 4; ++ch) {
        f32x2 acc[4], tp[4];
#pragma unroll
        for (int e = 0; e < 4; ++e) {
          const unsigned mag = (unsigned)(c * 16 + ch * 4 + e);
          const float tv = mag == TAB_ZERO ? 0.f : __uint_as_float((TAB_LO + mag) << 16);
          tp[e] = f32x2{tv, -tv}; acc[e] = f32x2{0.f, 0.f};
        }
#pragma unroll
        for (int i = 0; i < 7; ++i)
#pragma unroll
          for (int e = 0; e < 4; ++e) acc[e] = mlp_term(cf[i], tp[e], acc[e]);
        const float r0 = row16_sum(xrow_sum4(acc[0].x, acc[0].y, acc[1].x, acc[1].y));
        const float r1 = row16_sum(xrow_sum4(acc[2].x, acc[2].y, acc[3].x, acc[3].y));
        if (c16 == 0) { *(LAS float*)(L + L_MLP + hw * 128 + ch * 32 + g * 4) = r0; *(LAS float*)(L + L_MLP + hw * 128 + ch * 32 + 16 + g * 4) = r1; }
      }
      wait_lgkm();
      unsigned old = 0;
      if (lane == 0) old = __hip_atomic_fetch_add((LAS unsigned*)(L + L_CTL + 4 * C_TCNT), 1u, __ATOMIC_RELAXED, __HIP_MEMORY_SCOPE_WORKGROUP);
      old = __builtin_amdgcn_readfirstlane(old);
      asm volatile("" ::: "memory");
      if (old == 3u && lane < 16) {
        f32x2 y = {0.f, 0.f};
#pragma unroll
        for (int w = 0; w < 4; ++w) y += *(LAS f32x2*)(L + L_MLP + w * 128 + lane * 8);
        const unsigned yp = pk_bf16(y.x, y.y), tl = lds_ld(L, C_TLO), th = lds_ld(L, C_THI);
        const unsigned tlo = tl << 16, thi = (tl >> 16) | (th << 16);
        st_sc1(rws, WS_TAB + (unsigned)c * 256u + lane * 16u, v4u{(yp & 0xffffu) | tlo, thi, (yp >> 16) | tlo, thi});
      }
    }
    if (hw == 0) STAMP(10);
    lds_wait(L, C_WO + hw, 1u);
    v2u wf[8][8];
    {
      const v4i ro = mkrsrc(p.wo, HQ * DH * DM * 2);
      const unsigned stg = L_RING + hw * 32768 + 16384;
      const unsigned wbase = (unsigned)(kvh * 512 + hw * 128) * 1024u + (unsigned)sp * 256u;
      const unsigned w0 = g * 1024 + 16 * (c16 ^ (g << 1)), w1 = g * 1024 + 3072 + 16 * (c16 ^ ((4 + g) << 1));
#pragma unroll
      for (int rnd = 0; rnd < 2; ++rnd) {
#pragma unroll
        for (int q4 = 0; q4 < 4; ++q4) { const int ch = rnd * 4 + q4; dma4_w(ro, w0, w1, w0 + 6144, w1 + 6144, wbase + (unsigned)ch * 16384u, stg + q4 * 4096); }
        wait_vm<0>();
#pragma unroll
        for (int q4 = 0; q4 < 4; ++q4)
#pragma unroll
          for (int cc = 0; cc < 8; ++cc) wf[rnd * 4 + q4][cc] = tr_read(L + stg + q4 * 4096 + (ra ^ (unsigned)(cc << 5)));
        wait_lgkm();
      }
    }
    if (hw == 1 && (e16 % (32 / NFIN)) == 0) {
      const unsigned tl = lds_ld(L, C_TLO), th = lds_ld(L, C_THI);
      const unsigned tlo = tl << 16, thi = (tl >> 16) | (th << 16);
#pragma nounroll
      for (int bt = 0; bt < 8; ++bt) {
        v4u tv[8];
        for (unsigned s = 0; s < SPIN_MAX; ++s) {
          asm volatile("" ::: "memory");
          unsigned bad = 0u;
#pragma unroll
          for (int i = 0; i < 8; ++i) tv[i] = ld_sc1(rws, WS_TAB + (unsigned)(bt * 8 + i) * 1024u + lane * 16u);
#pragma unroll
          for (int i = 0; i < 8; ++i) bad |= (((tv[i].x ^ tlo) | (tv[i].z ^ tlo)) >> 16) | (tv[i].y ^ thi) | (tv[i].w ^ thi);
          if (__builtin_amdgcn_ballot_w64(bad != 0u) == 0ull) break;
          __builtin_amdgcn_s_sleep(64);
        }
#pragma unroll
        for (int i = 0; i < 8; ++i) *(LAS unsigned*)(L + L_STAGE + (bt * 8 + i) * 256 + lane * 4) = (tv[i].x & 0xffffu) | (tv[i].z << 16);
      }
      wait_lgkm();
      lds_st(L, C_TABOK, 1u);
    }
    if (hw == 3) {
      lds_wait(L, C_WO + 3, 1u);
      const u64 T48 = ((u64)lds_ld(L, C_TLO) | ((u64)lds_ld(L, C_THI) << 32)) << 16;
      const GAS u64* fp = (const GAS u64*)(p.ws + WS_Q) + b * 4096 + ((lane & 7) * 32 + 4 * b + ((lane >> 3) & 3)) * 16;
      for (unsigned s = 0; s < SPIN_MAX; ++s) {
        const u64 f = __hip_atomic_load(fp, __ATOMIC_RELAXED, __HIP_MEMORY_SCOPE_AGENT);
        if (__builtin_amdgcn_ballot_w64(((f ^ T48) >> 16) != 0ull) == 0ull) break;
        __builtin_amdgcn_s_sleep(64);
      }
      asm volatile("" ::: "memory");
      lds_st(L, C_FLAGOK, 1u);
    }
    lds_wait(L, C_FLAGOK, 1u);
    if (hw == 0) STAMP(6);
    lds_wait(L, C_MERGE, 2u);
    {
      const int sg = lane & 3, jq = (lane >> 2) & 3;
      const unsigned rec = WS_REC + (unsigned)(grp * 4 + sg) * REC_BYTES;
      const unsigned doff = rec + hw * 512 + (32 * jq + 4 * g) * 4;
      v4u d0, d1; v2u hd; unsigned ok = 0u;
      for (unsigned s = 0; s < SPIN_MAX; ++s) {
        asm volatile("" ::: "memory");
        if ((ok & 1u) == 0u) {
          hd = __builtin_amdgcn_raw_buffer_load_b64(rws, (int)(rec + 2048 + hw * 8), 0, 16);
          d0 = ld_sc1(rws, doff); d1 = ld_sc1(rws, doff + 64);
          ok = hd.x & hd.y & d0.x & d0.y & d0.z & d0.w & d1.x & d1.y & d1.z & d1.w;
        }
        if (__builtin_amdgcn_ballot_w64((ok & 1u) == 0u) == 0ull) break;
      }
      if (hw == 0) STAMP(11);
      const float mh = __uint_as_float(hd.x), lh = __uint_as_float(hd.y);
      float mx = fmaxf(mh, dpp<DPP_XOR1>(mh)); mx = fmaxf(mx, dpp<DPP_XOR2>(mx));
      const float e = __builtin_amdgcn_exp2f((mh - mx) * LOG2E);
      float lt = e * lh; lt += dpp<DPP_XOR1>(lt); lt += dpp<DPP_XOR2>(lt);
      const float w = e * __builtin_amdgcn_rcpf(lt);
      float ov[8];
#pragma unroll
      for (int i = 0; i < 4; ++i) { ov[i] = __uint_as_float(d0[i]) * w; ov[4 + i] = __uint_as_float(d1[i]) * w; }
#pragma unroll
      for (int i = 0; i < 8; ++i) { ov[i] += dpp<DPP_XOR1>(ov[i]); ov[i] += dpp<DPP_XOR2>(ov[i]); }
      const v4u af = {pk_bf16(ov[0], ov[1]), pk_bf16(ov[2], ov[3]), pk_bf16(ov[4], ov[5]), pk_bf16(ov[6], ov[7])};
      f32x4 acc[8];
#pragma unroll
      for (int cc = 0; cc < 8; ++cc) acc[cc] = f32x4{0.f, 0.f, 0.f, 0.f};
#pragma unroll
      for (int j = 0; j < 4; ++j) {
        const v4u aj = jq == j ? af : v4u{0u, 0u, 0u, 0u};
#pragma unroll
        for (int cc = 0; cc < 8; ++cc) acc[cc] = mfma32(aj, v4u{wf[2 * j][cc].x, wf[2 * j][cc].y, wf[2 * j + 1][cc].x, wf[2 * j + 1][cc].y}, acc[cc]);
      }
      *(LAS float*)(L + L_Y + hw * 512 + lane * 4) = xrow_sum4(acc[0][0], acc[1][0], acc[2][0], acc[3][0]);
      *(LAS float*)(L + L_Y + hw * 512 + 256 + lane * 4) = xrow_sum4(acc[4][0], acc[5][0], acc[6][0], acc[7][0]);
    }
    wait_lgkm();
    unsigned old = 0;
    if (lane == 0) old = __hip_atomic_fetch_add((LAS unsigned*)(L + L_CTL + 4 * C_YCNT), 1u, __ATOMIC_RELAXED, __HIP_MEMORY_SCOPE_WORKGROUP);
    old = __builtin_amdgcn_readfirstlane(old);
    asm volatile("" ::: "memory");
    if (old == 3u) {
      STAMP(7);
      if (lane < 32) {
        const f32x4 y0 = *(LAS f32x4*)(L + L_Y + lane * 16), y1 = *(LAS f32x4*)(L + L_Y + 512 + lane * 16);
        const f32x4 y2 = *(LAS f32x4*)(L + L_Y + 1024 + lane * 16), y3 = *(LAS f32x4*)(L + L_Y + 1536 + lane * 16);
        const f32x4 y = (y0 + y1) + (y2 + y3);
        st_sc1(rws, yslot + lane * 16, v4u{tagf(y[0]), tagf(y[1]), tagf(y[2]), tagf(y[3])});
      }
    }
  }
}

}

extern "C" void kernel_launch(void* const* d_in, const int* in_sizes, int n_in, void* d_out, int out_size, void* d_ws, size_t ws_size,
                              hipStream_t stream) {
  Params p;
  p.x = (const unsigned short*)d_in[0]; p.k = (const unsigned short*)d_in[1]; p.v = (const unsigned short*)d_in[2];
  p.wq = (const unsigned short*)d_in[3]; p.wo = (const unsigned short*)d_in[4];
  p.w1 = (const unsigned short*)d_in[6]; p.b1 = (const unsigned short*)d_in[7]; p.w2 = (const unsigned short*)d_in[8]; p.b2 = (const unsigned short*)d_in[9];
  p.out = (unsigned short*)d_out; p.ws = (unsigned char*)d_ws;
  (void)hipFuncSetAttribute((const void*)fused_decode_kernel, hipFuncAttributeMaxDynamicSharedMemorySize, (int)L_END + 128);
  hipLaunchKernelGGL(fused_decode_kernel, dim3(NWG), dim3(NTHR), L_END + 128, stream, p);
}
```

```cpp
#define X_WONT 1
#include <hip/hip_runtime.h>
#include <hip/hip_bf16.h>
#include <stdint.h>

#define LAS __attribute__((address_space(3)))
#define GAS __attribute__((address_space(1)))
typedef unsigned v4u __attribute__((ext_vector_type(4)));
typedef unsigned v2u __attribute__((ext_vector_type(2)));
typedef int v4i __attribute__((ext_vector_type(4)));
typedef float f32x4 __attribute__((ext_vector_type(4)));
typedef float f32x2 __attribute__((ext_vector_type(2)));
typedef short s16x4 __attribute__((ext_vector_type(4)));
typedef __bf16 bf16x8 __attribute__((ext_vector_type(8)));
typedef __bf16 bf16x2 __attribute__((ext_vector_type(2)));
typedef unsigned long long u64;

#define X_WQNT 0
#define X_QD 64
#define X_QS 32
#define STAMPS 0
#define TAB_EXP_LO 100u

namespace {

constexpr int NB = 8, DM = 512, HQ = 32, HKV = 8, DH = 128;
constexpr int NWG = 256, NTHR = 512;
constexpr int TILES = 32;
constexpr int NFIN = 4;
constexpr float LN_EPS = 1e-5f;
constexpr float SCALE = 0.08838834764831845f, LOG2E = 1.4426950408889634f;

constexpr unsigned WS_EPOCH = 0;
constexpr unsigned WS_Y = 8192;
constexpr unsigned WS_Q = WS_Y + NB * HKV * DM * 4;
constexpr unsigned REC_BYTES = 2304;
constexpr unsigned WS_REC = WS_Q + NB * 4096 * 8;
constexpr unsigned WS_TAB = WS_REC + NWG * REC_BYTES;
constexpr unsigned WS_STAMP = WS_TAB + 8192 * 8;
constexpr unsigned WS_END = WS_STAMP + NWG * 16 * 8;

constexpr unsigned L_RING = 0;
constexpr unsigned L_STAGE = 131072;
constexpr unsigned L_QBUF = L_STAGE + 16384;
constexpr unsigned L_PART = L_QBUF + 1024;
constexpr unsigned L_XN = L_PART, XN_ROW = 1040;
constexpr unsigned L_ML = L_PART + 8320;
constexpr unsigned L_Y = L_ML + 128;
constexpr unsigned L_T = L_Y + 2048;
constexpr unsigned L_MLP = L_T + 128;
constexpr unsigned L_CTL = L_MLP + 512;
constexpr unsigned L_END = L_CTL + 128;
static_assert(L_END + 128 <= 160 * 1024, "LDS budget");
enum { C_QRDY = 0, C_XN = 1, C_MERGE = 2, C_YCNT = 3, C_TLO = 4, C_THI = 5, C_TRDY = 6, C_NEAR = 7, C_FLAGOK = 8, C_TCNT = 9, C_TABOK = 10, C_WO = 12   };

constexpr unsigned SPIN_MAX = 1u << 18;

struct Params {
  const unsigned short* k; const unsigned short* v; const unsigned short* wo; unsigned char* ws;
  const unsigned short* x; const unsigned short* wq;
  const unsigned short* w1; const unsigned short* b1; const unsigned short* w2; const unsigned short* b2;
  unsigned short* out;
};

__device__ __forceinline__ v4i mkrsrc(const void* p, unsigned bytes) {
  const u64 a = (u64)p;
  v4i r; r.x = __builtin_amdgcn_readfirstlane((int)(unsigned)a); r.y = __builtin_amdgcn_readfirstlane((int)((unsigned)(a >> 32) & 0xffffu));
  r.z = (int)bytes; r.w = 0x00020000; return r;
}
__device__ __forceinline__ void dma4_nt(v4i rs, unsigned v0, unsigned v1, unsigned v2, unsigned v3, unsigned soff, unsigned lds) {
  asm volatile("s_mov_b32 m0, %6\n\ts_nop 0\n\t"
               "buffer_load_dwordx4 %0, %4, %5 offen nt lds\n\t"
               "buffer_load_dwordx4 %1, %4, %5 offen offset:1024 nt lds\n\t"
               "buffer_load_dwordx4 %2, %4, %5 offen offset:2048 nt lds\n\t"
               "buffer_load_dwordx4 %3, %4, %5 offen offset:3072 nt lds"
               :: "v"(v0), "v"(v1), "v"(v2), "v"(v3), "s"(rs), "s"(soff), "s"(lds) : "memory");
}
__device__ __forceinline__ void dma4_w(v4i rs, unsigned v0, unsigned v1, unsigned v2, unsigned v3, unsigned soff, unsigned lds) {
  asm volatile("s_mov_b32 m0, %6\n\ts_nop 0\n\t"
               "buffer_load_dwordx4 %0, %4, %5 offen lds\n\t"
               "buffer_load_dwordx4 %1, %4, %5 offen offset:1024 lds\n\t"
               "buffer_load_dwordx4 %2, %4, %5 offen offset:2048 lds\n\t"
               "buffer_load_dwordx4 %3, %4, %5 offen offset:3072 lds"
               :: "v"(v0), "v"(v1), "v"(v2), "v"(v3), "s"(rs), "s"(soff), "s"(lds) : "memory");
}
template <int N> __device__ __forceinline__ void wait_vm() { asm volatile("s_waitcnt vmcnt(%0)" :: "i"(N) : "memory"); }
__device__ __forceinline__ void wait_lgkm() { asm volatile("s_waitcnt lgkmcnt(0)" ::: "memory"); }
__device__ __forceinline__ void wg_barrier() { asm volatile("s_waitcnt lgkmcnt(0)\n\ts_barrier" ::: "memory"); }

template <int CTRL> __device__ __forceinline__ float dpp(float x) {
  return __builtin_bit_cast(float, __builtin_amdgcn_mov_dpp(__builtin_bit_cast(int, x), CTRL, 0xf, 0xf, true));
}
constexpr int DPP_XOR1 = 0xB1, DPP_XOR2 = 0x4E, DPP_HMIRROR = 0x141, DPP_ROR8 = 0x128;
__device__ __forceinline__ float xrow_max(float x) {
  auto s = __builtin_amdgcn_permlane16_swap(__float_as_uint(x), __float_as_uint(x), false, false);
  x = fmaxf(__uint_as_float(s[0]), __uint_as_float(s[1]));
  auto t = __builtin_amdgcn_permlane32_swap(__float_as_uint(x), __float_as_uint(x), false, false);
  return fmaxf(__uint_as_float(t[0]), __uint_as_float(t[1]));
}
__device__ __forceinline__ float xrow_sum(float x) {
  auto s = __builtin_amdgcn_permlane16_swap(__float_as_uint(x), __float_as_uint(x), false, false);
  x = __uint_as_float(s[0]) + __uint_as_float(s[1]);
  auto t = __builtin_amdgcn_permlane32_swap(__float_as_uint(x), __float_as_uint(x), false, false);
  return __uint_as_float(t[0]) + __uint_as_float(t[1]);
}
__device__ __forceinline__ float row16_sum(float x) {
  x += dpp<DPP_XOR1>(x); x += dpp<DPP_XOR2>(x); x += dpp<DPP_HMIRROR>(x); x += dpp<DPP_ROR8>(x); return x;
}
__device__ __forceinline__ float wave_sum(float x) { return xrow_sum(row16_sum(x)); }
__device__ __forceinline__ float xrow_sum4(float a, float b, float c, float d) {
  auto s01 = __builtin_amdgcn_permlane16_swap(__float_as_uint(a), __float_as_uint(b), false, false);
  auto s23 = __builtin_amdgcn_permlane16_swap(__float_as_uint(c), __float_as_uint(d), false, false);
  const float x = __uint_as_float(s01[0]) + __uint_as_float(s01[1]), y = __uint_as_float(s23[0]) + __uint_as_float(s23[1]);
  auto t = __builtin_amdgcn_permlane32_swap(__float_as_uint(x), __float_as_uint(y), false, false);
  return __uint_as_float(t[0]) + __uint_as_float(t[1]);
}
__device__ __forceinline__ unsigned pk_bf16(float lo, float hi) {
  bf16x2 r; r[0] = (__bf16)lo; r[1] = (__bf16)hi; return __builtin_bit_cast(unsigned, r);
}
__device__ __forceinline__ float bf_lo(unsigned u) { return __uint_as_float(u << 16); }
__device__ __forceinline__ float bf_hi(unsigned u) { return __uint_as_float(u & 0xffff0000u); }
__device__ __forceinline__ float bf1(unsigned short u) { return __uint_as_float((unsigned)u << 16); }
__device__ __forceinline__ float rbf(float x) { return bf_lo(pk_bf16(x, x)); }
__device__ __forceinline__ f32x2 rbf2(f32x2 v) {
  unsigned u; asm("v_cvt_pk_bf16_f32 %0, %1, %2" : "=v"(u) : "v"(v.x), "v"(v.y));
  return f32x2{bf_lo(u), bf_hi(u)};
}
__device__ __forceinline__ f32x4 mfma32(v4u a, v4u b, f32x4 c) {
  return __builtin_amdgcn_mfma_f32_16x16x32_bf16(__builtin_bit_cast(bf16x8, a), __builtin_bit_cast(bf16x8, b), c, 0, 0, 0);
}
__device__ __forceinline__ f32x4 mfma16(v2u a, v2u b, f32x4 c) {
  return __builtin_amdgcn_mfma_f32_16x16x16bf16_1k(__builtin_bit_cast(s16x4, a), __builtin_bit_cast(s16x4, b), c, 0, 0, 0);
}
__device__ __forceinline__ v2u tr_read(LAS unsigned char* p) {
  return __builtin_bit_cast(v2u, __builtin_amdgcn_ds_read_tr16_b64_v4i16((LAS s16x4*)p));
}
__device__ __forceinline__ v4u ld_sc1(__amdgpu_buffer_rsrc_t rs, unsigned off) { return __builtin_amdgcn_raw_buffer_load_b128(rs, (int)off, 0, 16); }
__device__ __forceinline__ void st_sc1(__amdgpu_buffer_rsrc_t rs, unsigned off, v4u v) { __builtin_amdgcn_raw_buffer_store_b128(v, rs, (int)off, 0, 16); }
__device__ __forceinline__ unsigned lds_ld(LAS unsigned char* L, int idx) { return *(volatile LAS unsigned*)(L + L_CTL + 4 * idx); }
__device__ __forceinline__ void lds_st(LAS unsigned char* L, int idx, unsigned v) { *(volatile LAS unsigned*)(L + L_CTL + 4 * idx) = v; }
__device__ __forceinline__ void lds_wait(LAS unsigned char* L, int idx, unsigned want) {
  for (unsigned s = 0; s < SPIN_MAX; ++s) { if (lds_ld(L, idx) >= want) break; __builtin_amdgcn_s_sleep(1); }
  asm volatile("" ::: "memory");
}
__device__ __forceinline__ unsigned tagf(float f) { return __float_as_uint(f) | 1u; }
#define STAMP(i) do { } while (0)

struct AttnState { float m, l; f32x4 o[8]; };

constexpr int slot_of(int n) { return n < 24 ? (n & 7) : (n & 3); }
struct StreamCtx {
  LAS unsigned char* L; unsigned ring; v4i rk, rv; unsigned vk[4], vv[2], ka[4], va[8]; unsigned tbase;
  template <int M> __device__ __forceinline__ void issue(int j) const {
    if constexpr ((M & 1) == 0) dma4_nt(rk, vk[0], vk[1], vk[2], vk[3], tbase + (unsigned)j * 65536u, ring + slot_of(M) * 4096);
    else dma4_nt(rv, vv[0], vv[1], vv[0], vv[1], tbase + (unsigned)j * 65536u, ring + slot_of(M) * 4096);
  }
  template <int N> __device__ __forceinline__ void step(int t) const {
    wait_vm<(N + 1 < 2 * TILES) ? 4 : 0>();
    if constexpr (N + 2 < 2 * TILES) issue<N + 2>(t + 1);
  }
};

__device__ __forceinline__ void softmax_pv(AttnState& st, const f32x4 sc, const v2u (&vf)[8], bool lds_pending) {
  const unsigned r01 = pk_bf16(sc[0], sc[1]), r23 = pk_bf16(sc[2], sc[3]);
  const unsigned u01 = pk_bf16(bf_lo(r01) * SCALE, bf_hi(r01) * SCALE), u23 = pk_bf16(bf_lo(r23) * SCALE, bf_hi(r23) * SCALE);
  const float s0 = bf_lo(u01), s1 = bf_hi(u01), s2 = bf_lo(u23), s3 = bf_hi(u23);
  const float tm = xrow_max(fmaxf(fmaxf(s0, s1), fmaxf(s2, s3)));
  if (__builtin_amdgcn_ballot_w64(tm > st.m) != 0ull) {
    const float mn = fmaxf(st.m, tm);
    const float al = __builtin_amdgcn_exp2f((st.m - mn) * LOG2E);
    st.m = mn; st.l *= al;
    const float a0 = dpp<0x00>(al), a1 = dpp<0x55>(al), a2 = dpp<0xAA>(al), a3 = dpp<0xFF>(al);
#pragma unroll
    for (int c = 0; c < 8; ++c) { st.o[c][0] *= a0; st.o[c][1] *= a1; st.o[c][2] *= a2; st.o[c][3] *= a3; }
  }
  const float p0 = __builtin_amdgcn_exp2f((s0 - st.m) * LOG2E), p1 = __builtin_amdgcn_exp2f((s1 - st.m) * LOG2E);
  const float p2 = __builtin_amdgcn_exp2f((s2 - st.m) * LOG2E), p3 = __builtin_amdgcn_exp2f((s3 - st.m) * LOG2E);
  st.l += (p0 + p1) + (p2 + p3);
  v2u pf; pf.x = pk_bf16(p0, p1); pf.y = pk_bf16(p2, p3);
  if (lds_pending) wait_lgkm();
#pragma unroll
  for (int c = 0; c < 8; ++c) st.o[c] = mfma16(pf, vf[c], st.o[c]);
}
struct TileRegs { v4u kf[4]; v2u vf[8]; };
template <int TC> __device__ __forceinline__ void read_k(TileRegs& tr, const StreamCtx& cx) {
#pragma unroll
  for (int s = 0; s < 4; ++s) tr.kf[s] = *(LAS v4u*)(cx.L + cx.ring + slot_of(2 * TC) * 4096 + cx.ka[s]);
}
template <int TC> __device__ __forceinline__ void read_v(TileRegs& tr, const StreamCtx& cx) {
#pragma unroll
  for (int c = 0; c < 8; ++c) tr.vf[c] = tr_read(cx.L + cx.ring + slot_of(2 * TC) * 4096 + 4096 + cx.va[c]);
}
__device__ __forceinline__ f32x4 qk(const TileRegs& tr, const v4u (&qf)[4]) {
  f32x4 sc = {0.f, 0.f, 0.f, 0.f};
#pragma unroll
  for (int s = 0; s < 4; ++s) sc = mfma32(tr.kf[s], qf[s], sc);
  return sc;
}
template <int TC> __device__ __forceinline__ void attn_tile(AttnState& st, const StreamCtx& cx, const v4u (&qf)[4], int t) {
  TileRegs tr;
  cx.step<2 * TC>(t); read_k<TC>(tr, cx);
  const f32x4 sc = qk(tr, qf);
  cx.step<2 * TC + 1>(t); read_v<TC>(tr, cx);
  softmax_pv(st, sc, tr.vf, true);
}
template <int TC> __device__ __forceinline__ void ring_tile(AttnState& st, const StreamCtx& cx, const v4u (&qf)[4]) {
  TileRegs tr; read_k<TC>(tr, cx); read_v<TC>(tr, cx);
  softmax_pv(st, qk(tr, qf), tr.vf, true);
}
template <int TC> __device__ __forceinline__ void park_tile(TileRegs& tr, const StreamCtx& cx) {
  cx.step<2 * TC>(TC); read_k<TC>(tr, cx);
  cx.step<2 * TC + 1>(TC); read_v<TC>(tr, cx);
  wait_lgkm();
}
__device__ __forceinline__ void parked_tile(AttnState& st, const TileRegs& tr, const v4u (&qf)[4]) { softmax_pv(st, qk(tr, qf), tr.vf, false); }

__device__ __forceinline__ void ln_stats(const f32x2 (&z)[4], float& mb, float& rs) {
  const float pv = __builtin_bit_cast(float, __builtin_amdgcn_readfirstlane(__builtin_bit_cast(int, z[0].x)));
  f32x2 s = {0.f, 0.f}, q = {0.f, 0.f};
#pragma unroll
  for (int e = 0; e < 4; ++e) { const f32x2 d = z[e] - f32x2{pv, pv}; s += d; q = __builtin_elementwise_fma(d, d, q); }
  float s1 = s.x + s.y, q1 = q.x + q.y;
  s1 += dpp<DPP_XOR1>(s1); q1 += dpp<DPP_XOR1>(q1); s1 += dpp<DPP_XOR2>(s1); q1 += dpp<DPP_XOR2>(q1);
  s1 += dpp<DPP_HMIRROR>(s1); q1 += dpp<DPP_HMIRROR>(q1); s1 += dpp<DPP_ROR8>(s1); q1 += dpp<DPP_ROR8>(q1);
  const float dm = xrow_sum(s1) * (1.f / DM), q2 = xrow_sum(q1) * (1.f / DM);
  const float var = fmaxf(fmaf(-dm, dm, q2), 0.f);
  mb = rbf(pv + dm);
  rs = __builtin_amdgcn_rcpf(rbf(__builtin_amdgcn_sqrtf(rbf(rbf(var) + LN_EPS))));
}
__device__ __forceinline__ f32x2 ln_apply(f32x2 z, float mb, float rs) { return rbf2(rbf2(z - f32x2{mb, mb}) * f32x2{rs, rs}); }

constexpr float GELU_K0 = -2.f * 0.7978845608028654f * LOG2E, GELU_K1 = GELU_K0 * 0.044715f;
struct MlpCoef { float k3, k2, k1, k0, wa, wb; };
__device__ __forceinline__ MlpCoef mlp_coef(unsigned short w1r, unsigned short b1r, unsigned short w2r) {
  constexpr float K0 = GELU_K0, K1 = GELU_K1;
  const float a = bf1(w1r), bb = bf1(b1r), w2 = bf1(w2r);
  MlpCoef c;
  c.k3 = K1 * a * a * a; c.k2 = 3.f * K1 * a * a * bb; c.k1 = a * fmaf(3.f * K1 * bb, bb, K0); c.k0 = bb * fmaf(K1 * bb, bb, K0);
  c.wa = w2 * a; c.wb = w2 * bb;
  return c;
}
__device__ __forceinline__ f32x2 mlp_term(const MlpCoef& c, f32x2 t, f32x2 acc) {
  f32x2 ar = __builtin_elementwise_fma(f32x2{c.k3, c.k3}, t, f32x2{c.k2, c.k2});
  ar = __builtin_elementwise_fma(ar, t, f32x2{c.k1, c.k1});
  ar = __builtin_elementwise_fma(ar, t, f32x2{c.k0, c.k0});
  const f32x2 wu = __builtin_elementwise_fma(f32x2{c.wa, c.wa}, t, f32x2{c.wb, c.wb});
  f32x2 d = {__builtin_amdgcn_exp2f(ar.x), __builtin_amdgcn_exp2f(ar.y)};
  d += f32x2{1.f, 1.f};
  const f32x2 r = {__builtin_amdgcn_rcpf(d.x), __builtin_amdgcn_rcpf(d.y)};
  return __builtin_elementwise_fma(wu, r, acc);
}
constexpr unsigned TAB_LO = TAB_EXP_LO << 7, TAB_N = 4096u, TAB_ZERO = TAB_N - 1u;

__global__ void __launch_bounds__(NTHR, 2) fused_decode_kernel(Params p) {
  extern __shared__ __attribute__((aligned(16))) unsigned char lds_raw[];
  LAS unsigned char* L = (LAS unsigned char*)lds_raw;
  const int tid = threadIdx.x, lane = tid & 63, wave = __builtin_amdgcn_readfirstlane(tid >> 6);
  const int c = blockIdx.x;
  const int b = c >> 5, kvh = c & 7, sp = (c >> 3) & 3, grp = b * 8 + kvh;
  const int rid = grp * 4 + sp;
  const int cq = (c & 7) * 32 + (c >> 3);
  const int e16 = c & 31;
  const int g = lane >> 4, c16 = lane & 15;
  const __amdgpu_buffer_rsrc_t rws = __builtin_amdgcn_make_buffer_rsrc(p.ws, 0, WS_END, 0x00020000);

  const unsigned ra = 256 * (4 * g + (c16 >> 2)) + 16 * (((lane >> 1) & 1) ^ (((4 * g + (c16 >> 2)) & 7) << 1)) + 8 * (lane & 1);

  if (wave < 4) {
    if (wave == 0) STAMP(0);
    __builtin_amdgcn_s_setprio(2);
    StreamCtx cx;
    cx.L = L; cx.rk = mkrsrc(p.k, 1u << 27); cx.rv = mkrsrc(p.v, 1u << 27);
#pragma unroll
    for (int i = 0; i < 4; ++i) cx.vk[i] = g * 256 + 16 * (c16 ^ (4 * i + g));
#pragma unroll
    for (int i = 0; i < 2; ++i) cx.vv[i] = g * 256 + 16 * (c16 ^ (((4 * i + g) & 7) << 1));
    cx.ring = L_RING + wave * 32768;
    cx.tbase = (unsigned)grp * (2048u * 1024u) + (unsigned)(sp * 4 + wave) * 4096u;
    cx.issue<0>(0); cx.issue<1>(0);
    wg_barrier();
#pragma unroll
    for (int s = 0; s < 4; ++s) cx.ka[s] = 256 * c16 + 16 * ((4 * s + g) ^ c16);
#pragma unroll
    for (int cc = 0; cc < 8; ++cc) cx.va[cc] = ra ^ (unsigned)(cc << 5);
    TileRegs pk0, pk1, pk2, pk3;
    park_tile<0>(pk0, cx); park_tile<1>(pk1, cx); park_tile<2>(pk2, cx); park_tile<3>(pk3, cx);
    cx.step<8>(4); cx.step<9>(4); cx.step<10>(5);
    if (wave == 0) STAMP(12);
    lds_wait(L, C_QRDY, 1u);
    v4u qf[4];
#pragma unroll
    for (int s = 0; s < 4; ++s) qf[s] = *(LAS v4u*)(L + L_QBUF + (lane & 3) * 256 + s * 64 + g * 16);
    AttnState st; st.m = -INFINITY; st.l = 0.f;
#pragma unroll
    for (int cc = 0; cc < 8; ++cc) st.o[cc] = f32x4{0.f, 0.f, 0.f, 0.f};
    parked_tile(st, pk0, qf);
    cx.step<11>(5); parked_tile(st, pk1, qf);
    cx.step<12>(6); parked_tile(st, pk2, qf);
    cx.step<13>(6); parked_tile(st, pk3, qf);
    ring_tile<4>(st, cx, qf); cx.step<14>(7);
    cx.step<15>(7); ring_tile<5>(st, cx, qf);
    cx.step<16>(8); ring_tile<6>(st, cx, qf);
    cx.step<17>(8); ring_tile<7>(st, cx, qf);
    cx.step<18>(9); ring_tile<8>(st, cx, qf);
    cx.step<19>(9); ring_tile<9>(st, cx, qf);
    attn_tile<10>(st, cx, qf, 10); attn_tile<11>(st, cx, qf, 11);
    lds_st(L, C_WO + wave, 1u);
    if (wave == 0) STAMP(13);
    for (int j = 12; j < TILES - 2; j += 2) { attn_tile<12>(st, cx, qf, j); attn_tile<13>(st, cx, qf, j + 1); }
    attn_tile<30>(st, cx, qf, 30); attn_tile<31>(st, cx, qf, 31);
    if (wave == 0) STAMP(2);
    const float lsum = xrow_sum(st.l);
#pragma unroll
    for (int cc = 0; cc < 8; ++cc) {
      const float v = g == 0 ? st.o[cc][0] : g == 1 ? st.o[cc][1] : g == 2 ? st.o[cc][2] : st.o[cc][3];
      *(LAS float*)(L + L_PART + wave * 2048 + g * 512 + (16 * cc + c16) * 4) = v;
    }
    if (lane < 4) *(LAS v2u*)(L + L_ML + wave * 32 + lane * 8) = v2u{__float_as_uint(st.m), __float_as_uint(lsum)};
    wait_lgkm();
    unsigned old = 0;
    if (lane == 0) old = __hip_atomic_fetch_add((LAS unsigned*)(L + L_CTL + 4 * C_MERGE), 1u, __ATOMIC_RELAXED, __HIP_MEMORY_SCOPE_WORKGROUP);
    old = __builtin_amdgcn_readfirstlane(old);
    asm volatile("" ::: "memory");
    if (old == 3u) {
      float mw[4], lw[4];
#pragma unroll
      for (int w = 0; w < 4; ++w) { const v2u ml = *(LAS v2u*)(L + L_ML + w * 32 + g * 8); mw[w] = __uint_as_float(ml.x); lw[w] = __uint_as_float(ml.y); }
      const float M = fmaxf(fmaxf(mw[0], mw[1]), fmaxf(mw[2], mw[3]));
      float Ls = 0.f; f32x4 a0 = {0.f, 0.f, 0.f, 0.f}, a1 = {0.f, 0.f, 0.f, 0.f};
#pragma unroll
      for (int w = 0; w < 4; ++w) {
        const float f = __builtin_amdgcn_exp2f((mw[w] - M) * LOG2E);
        const f32x4 x0 = *(LAS f32x4*)(L + L_PART + w * 2048 + g * 512 + c16 * 16), x1 = *(LAS f32x4*)(L + L_PART + w * 2048 + g * 512 + 256 + c16 * 16);
        Ls = fmaf(f, lw[w], Ls); a0 += f * x0; a1 += f * x1;
      }
      const unsigned rec = WS_REC + (unsigned)rid * REC_BYTES;
      st_sc1(rws, rec + g * 512 + c16 * 16, v4u{tagf(a0[0]), tagf(a0[1]), tagf(a0[2]), tagf(a0[3])});
      st_sc1(rws, rec + g * 512 + 256 + c16 * 16, v4u{tagf(a1[0]), tagf(a1[1]), tagf(a1[2]), tagf(a1[3])});
      if (c16 == 0) __builtin_amdgcn_raw_buffer_store_b64(v2u{tagf(M), tagf(Ls)}, rws, (int)(rec + 2048 + g * 8), 0, 16);
      STAMP(3);
    }
    if (wave == 0 && (e16 % (32 / NFIN)) == 0) {
      const unsigned short b2r = p.b2[0];
      const v2u xr0 = *(const GAS v2u*)(p.x + b * DM + lane * 4), xr1 = *(const GAS v2u*)(p.x + b * DM + 256 + lane * 4);
      lds_wait(L, C_FLAGOK, 1u);
      lds_wait(L, C_TABOK, 1u);
      const unsigned tl = lds_ld(L, C_TLO), th = lds_ld(L, C_THI);
      const unsigned yoff = WS_Y + (unsigned)b * 16384u + lane * 16;
      v4u yv[16]; unsigned pend = 0xffffu;
      for (unsigned s = 0; s < SPIN_MAX; ++s) {
        asm volatile("" ::: "memory");
#pragma unroll
        for (int i = 0; i < 16; ++i) if ((pend >> i) & 1u) yv[i] = ld_sc1(rws, yoff + i * 1024);
#pragma unroll
        for (int i = 0; i < 16; ++i) if ((pend >> i) & 1u) {
          const unsigned ok = yv[i].x & yv[i].y & yv[i].z & yv[i].w;
          if (__builtin_amdgcn_ballot_w64((ok & 1u) == 0u) == 0ull) pend &= ~(1u << i);
        }
        if (pend == 0u) break;
        if (__builtin_popcount(pend) > 4) __builtin_amdgcn_s_sleep(1);
      }
      STAMP(14);
      f32x2 hv[4], z[4];
#pragma unroll
      for (int hf = 0; hf < 2; ++hf) {
        f32x4 ys[8];
#pragma unroll
        for (int kk = 0; kk < 8; ++kk) ys[kk] = __builtin_bit_cast(f32x4, yv[2 * kk + hf]);
        const f32x4 y = ((ys[0] + ys[1]) + (ys[2] + ys[3])) + ((ys[4] + ys[5]) + (ys[6] + ys[7]));
        const v2u xr = hf ? xr1 : xr0;
        hv[2 * hf] = rbf2(f32x2{bf_lo(xr.x), bf_hi(xr.x)} + rbf2(f32x2{y[0], y[1]}));
        hv[2 * hf + 1] = rbf2(f32x2{bf_lo(xr.y), bf_hi(xr.y)} + rbf2(f32x2{y[2], y[3]}));
      }
      float mb, rs;
      ln_stats(hv, mb, rs);
#pragma unroll
      for (int e = 0; e < 4; ++e) z[e] = ln_apply(hv[e], mb, rs);
      ln_stats(z, mb, rs);
      STAMP(8);
      const float b2f = bf1(b2r);
#pragma unroll
      for (int hf = 0; hf < 2; ++hf) {
        if (NFIN > 1 && (e16 >> 4) != hf) continue;
        constexpr int NL = NFIN >= 2 ? 128 / NFIN : 64;
        const int l0 = NFIN >= 2 ? (4 * e16) & 63 : 0;
        const bool mine = lane >= l0 && lane < l0 + NL;
        const f32x2 za = ln_apply(z[2 * hf], mb, rs), zb = ln_apply(z[2 * hf + 1], mb, rs);
        const float tz[4] = {za.x, za.y, zb.x, zb.y}, h4[4] = {hv[2 * hf].x, hv[2 * hf].y, hv[2 * hf + 1].x, hv[2 * hf + 1].y};
        unsigned idx[4]; bool oot[4];
#pragma unroll
        for (int e = 0; e < 4; ++e) {
          const unsigned bits = __float_as_uint(tz[e]) >> 16, mag = bits & 0x7fffu;
          const unsigned rel = mag == 0u ? TAB_ZERO : mag - TAB_LO;
          const bool outside = mag != 0u && rel >= TAB_ZERO;
          oot[e] = mine && outside;
          idx[e] = outside ? 0u : rel * 2u + (bits >> 15);
        }
        float y0 = bf1(*(LAS unsigned short*)(L + L_STAGE + idx[0] * 2u)), y1 = bf1(*(LAS unsigned short*)(L + L_STAGE + idx[1] * 2u));
        float y2 = bf1(*(LAS unsigned short*)(L + L_STAGE + idx[2] * 2u)), y3 = bf1(*(LAS unsigned short*)(L + L_STAGE + idx[3] * 2u));
        if (__builtin_amdgcn_ballot_w64(oot[0] || oot[1] || oot[2] || oot[3]) != 0ull) {
#pragma nounroll
          for (int e = 0; e < 4; ++e) {
            const float te = e == 0 ? tz[0] : e == 1 ? tz[1] : e == 2 ? tz[2] : tz[3];
            u64 todo = __builtin_amdgcn_ballot_w64(e == 0 ? oot[0] : e == 1 ? oot[1] : e == 2 ? oot[2] : oot[3]);
            while (todo != 0ull) {
              const int src = __builtin_ctzll(todo); todo &= todo - 1ull;
              const float tv = __builtin_bit_cast(float, __builtin_amdgcn_readlane(__builtin_bit_cast(int, te), src));
              float acc = 0.f;
#pragma nounroll
              for (int i = 0; i < 28; ++i) {
                const float u = fmaf(tv, bf1(p.w1[lane + 64 * i]), bf1(p.b1[lane + 64 * i]));
                const float ex = __builtin_amdgcn_exp2f(u * fmaf(u * u, GELU_K1, GELU_K0));
                acc = fmaf(u * bf1(p.w2[lane + 64 * i]), __builtin_amdgcn_rcpf(1.f + ex), acc);
              }
              const float yd = rbf(wave_sum(acc));
              if (lane == src) { if (e == 0) y0 = yd; else if (e == 1) y1 = yd; else if (e == 2) y2 = yd; else y3 = yd; }
            }
          }
        }
        if (mine) {
          const v2u o = {pk_bf16(h4[0] + rbf(y0 + b2f), h4[1] + rbf(y1 + b2f)), pk_bf16(h4[2] + rbf(y2 + b2f), h4[3] + rbf(y3 + b2f))};
          *(GAS v2u*)(p.out + b * DM + hf * 256 + lane * 4) = o;
        }
      }
      if (c == 0 && lane == 0) __hip_atomic_store((GAS u64*)(p.ws + WS_EPOCH), (u64)tl | ((u64)th << 32), __ATOMIC_RELAXED, __HIP_MEMORY_SCOPE_AGENT);
      STAMP(9);
    }
  } else {
    const int hw = wave - 4;
    const unsigned yslot = WS_Y + (((unsigned)b * 8u + (unsigned)kvh) * 512u + (unsigned)sp * 128u) * 4u;
    if (hw == 0) {
      const v4i rq = mkrsrc(p.wq, DM * HQ * DH * 2);
      const unsigned vo = (lane >> 1) * 8192 + (lane & 1) * 16;
#pragma unroll
      for (int s4 = 0; s4 < 4; ++s4)
        if (X_WQNT) dma4_nt(rq, vo, vo + 32u * 8192u - 1024u, vo + 64u * 8192u - 2048u, vo + 96u * 8192u - 3072u, (unsigned)cq * 32u + (unsigned)s4 * (128u * 8192u), L_STAGE + s4 * 4096); else dma4_w(rq, vo, vo + 32u * 8192u - 1024u, vo + 64u * 8192u - 2048u, vo + 96u * 8192u - 3072u, (unsigned)cq * 32u + (unsigned)s4 * (128u * 8192u), L_STAGE + s4 * 4096);
      const u64 ep = __hip_atomic_load((GAS u64*)(p.ws + WS_EPOCH), __ATOMIC_RELAXED, __HIP_MEMORY_SCOPE_AGENT);
      wg_barrier();
      const u64 T = ep + 0x9E3779B97F4A7C15ull;
      if (lane == 0) { lds_st(L, C_TLO, (unsigned)T); lds_st(L, C_THI, (unsigned)(T >> 32)); }
      wait_vm<0>();
      STAMP(1);
      lds_wait(L, C_XN, 3u);
      f32x4 d = {0.f, 0.f, 0.f, 0.f};
      const unsigned ta = (8 * g + (c16 >> 2)) * 32 + (lane & 3) * 8;
#pragma unroll
      for (int s = 0; s < 16; ++s) {
        const v2u lo = tr_read(L + L_STAGE + s * 1024 + ta), hi = tr_read(L + L_STAGE + s * 1024 + ta + 128);
        const v4u xf = *(LAS v4u*)(L + L_XN + (c16 & 7) * XN_ROW + s * 64 + g * 16);
        d = mfma32(v4u{lo.x, lo.y, hi.x, hi.y}, xf, d);
      }
      const unsigned p0 = pk_bf16(d[0], d[1]), p1 = pk_bf16(d[2], d[3]);
      const int src = (c16 + 16 * (2 * (c16 >> 3) + (g >> 1))) * 4;
      const unsigned q0 = (unsigned)__builtin_amdgcn_ds_bpermute(src, (int)p0), q1 = (unsigned)__builtin_amdgcn_ds_bpermute(src, (int)p1);
      const unsigned qp = (g & 1) ? q1 : q0;
      const unsigned tlo = (unsigned)T << 16, thi = (unsigned)(T >> 16);
      st_sc1(rws, WS_Q + ((unsigned)(c16 & 7) * 4096u + (unsigned)cq * 16u) * 8u + (4 * (c16 >> 3) + g) * 16u,
             v4u{(qp & 0xffffu) | tlo, thi, (qp >> 16) | tlo, thi});
      STAMP(4);
      const unsigned qoff = WS_Q + ((unsigned)b * 4096u + (unsigned)kvh * 512u) * 8u + lane * 16u;
      v4u a0, a1, a2, a3; unsigned bad = 1u;
      __builtin_amdgcn_s_sleep(X_QD);
      for (unsigned s = 0; s < SPIN_MAX; ++s) {
        asm volatile("" ::: "memory");
        if (bad != 0u) {
          a0 = ld_sc1(rws, qoff); a1 = ld_sc1(rws, qoff + 1024); a2 = ld_sc1(rws, qoff + 2048); a3 = ld_sc1(rws, qoff + 3072);
          bad = (((a0.x ^ tlo) | (a0.z ^ tlo) | (a1.x ^ tlo) | (a1.z ^ tlo) | (a2.x ^ tlo) | (a2.z ^ tlo) | (a3.x ^ tlo) | (a3.z ^ tlo)) >> 16) |
                (a0.y ^ thi) | (a0.w ^ thi) | (a1.y ^ thi) | (a1.w ^ thi) | (a2.y ^ thi) | (a2.w ^ thi) | (a3.y ^ thi) | (a3.w ^ thi);
        }
        if (__builtin_amdgcn_ballot_w64(bad != 0u) == 0ull) break;
        __builtin_amdgcn_s_sleep(X_QS);
      }
      *(LAS unsigned*)(L + L_QBUF + lane * 4) = (a0.x & 0xffffu) | (a0.z << 16);
      *(LAS unsigned*)(L + L_QBUF + 256 + lane * 4) = (a1.x & 0xffffu) | (a1.z << 16);
      *(LAS unsigned*)(L + L_QBUF + 512 + lane * 4) = (a2.x & 0xffffu) | (a2.z << 16);
      *(LAS unsigned*)(L + L_QBUF + 768 + lane * 4) = (a3.x & 0xffffu) | (a3.z << 16);
      wait_lgkm();
      lds_st(L, C_QRDY, 1u);
      STAMP(5);
    } else if (hw != 3) {
      v4u xv[4];
#pragma unroll
      for (int i = 0; i < 4; ++i) xv[i] = *((const GAS v4u*)(p.x + ((hw - 1) * 4 + i) * DM) + lane);
      wg_barrier();
#pragma unroll
      for (int i = 0; i < 4; ++i) {
        f32x2 z[4]; float mb, rs; v4u o;
#pragma unroll
        for (int e = 0; e < 4; ++e) z[e] = f32x2{bf_lo(xv[i][e]), bf_hi(xv[i][e])};
        ln_stats(z, mb, rs);
#pragma unroll
        for (int e = 0; e < 4; ++e) { const f32x2 t = rbf2(z[e] - f32x2{mb, mb}) * f32x2{rs, rs}; o[e] = pk_bf16(t.x, t.y); }
        *(LAS v4u*)(L + L_XN + ((hw - 1) * 4 + i) * XN_ROW + lane * 16) = o;
      }
      wait_lgkm();
      if (lane == 0) __hip_atomic_fetch_add((LAS unsigned*)(L + L_CTL + 4 * C_XN), 1u, __ATOMIC_RELAXED, __HIP_MEMORY_SCOPE_WORKGROUP);
    } else {
      const v4u z = {0u, 0u, 0u, 0u};
      const unsigned rec = WS_REC + (unsigned)rid * REC_BYTES;
      st_sc1(rws, rec + lane * 16, z); st_sc1(rws, rec + 1024 + lane * 16, z);
      if (lane < 2) st_sc1(rws, rec + 2048 + lane * 16, z);
      if (lane < 32) st_sc1(rws, yslot + lane * 16, z);
      if (lane < 32) lds_st(L, lane, 0u);
      wg_barrier();
      wait_vm<0>();
      if (lane == 0) __hip_atomic_fetch_add((LAS unsigned*)(L + L_CTL + 4 * C_XN), 1u, __ATOMIC_RELAXED, __HIP_MEMORY_SCOPE_WORKGROUP);
    }
    {
      MlpCoef cf[7];
      const int f0 = tid & 255;
#pragma unroll
      for (int i = 0; i < 7; ++i) cf[i] = mlp_coef(p.w1[f0 + 256 * i], p.b1[f0 + 256 * i], p.w2[f0 + 256 * i]);
#pragma nounroll
      for (int ch = 0; ch < 4; ++ch) {
        f32x2 acc[4], tp[4];
#pragma unroll
        for (int e = 0; e < 4; ++e) {
          const unsigned mag = (unsigned)(c * 16 + ch * 4 + e);
          const float tv = mag == TAB_ZERO ? 0.f : __uint_as_float((TAB_LO + mag) << 16);
          tp[e] = f32x2{tv, -tv}; acc[e] = f32x2{0.f, 0.f};
        }
#pragma unroll
        for (int i = 0; i < 7; ++i)
#pragma unroll
          for (int e = 0; e < 4; ++e) acc[e] = mlp_term(cf[i], tp[e], acc[e]);
        const float r0 = row16_sum(xrow_sum4(acc[0].x, acc[0].y, acc[1].x, acc[1].y));
        const float r1 = row16_sum(xrow_sum4(acc[2].x, acc[2].y, acc[3].x, acc[3].y));
        if (c16 == 0) { *(LAS float*)(L + L_MLP + hw * 128 + ch * 32 + g * 4) = r0; *(LAS float*)(L + L_MLP + hw * 128 + ch * 32 + 16 + g * 4) = r1; }
      }
      wait_lgkm();
      unsigned old = 0;
      if (lane == 0) old = __hip_atomic_fetch_add((LAS unsigned*)(L + L_CTL + 4 * C_TCNT), 1u, __ATOMIC_RELAXED, __HIP_MEMORY_SCOPE_WORKGROUP);
      old = __builtin_amdgcn_readfirstlane(old);
      asm volatile("" ::: "memory");
      if (old == 3u && lane < 16) {
        f32x2 y = {0.f, 0.f};
#pragma unroll
        for (int w = 0; w < 4; ++w) y += *(LAS f32x2*)(L + L_MLP + w * 128 + lane * 8);
        const unsigned yp = pk_bf16(y.x, y.y), tl = lds_ld(L, C_TLO), th = lds_ld(L, C_THI);
        const unsigned tlo = tl << 16, thi = (tl >> 16) | (th << 16);
        st_sc1(rws, WS_TAB + (unsigned)c * 256u + lane * 16u, v4u{(yp & 0xffffu) | tlo, thi, (yp >> 16) | tlo, thi});
      }
    }
    if (hw == 0) STAMP(10);
    lds_wait(L, C_WO + hw, 1u);
    v2u wf[8][8];
    {
      const v4i ro = mkrsrc(p.wo, HQ * DH * DM * 2);
      const unsigned stg = L_RING + hw * 32768 + 16384;
      const unsigned wbase = (unsigned)(kvh * 512 + hw * 128) * 1024u + (unsigned)sp * 256u;
      const unsigned w0 = g * 1024 + 16 * (c16 ^ (g << 1)), w1 = g * 1024 + 3072 + 16 * (c16 ^ ((4 + g) << 1));
#pragma unroll
      for (int rnd = 0; rnd < 2; ++rnd) {
#pragma unroll
        for (int q4 = 0; q4 < 4; ++q4) { const int ch = rnd * 4 + q4; if (X_WONT) dma4_nt(ro, w0, w1, w0 + 6144, w1 + 6144, wbase + (unsigned)ch * 16384u, stg + q4 * 4096); else dma4_w(ro, w0, w1, w0 + 6144, w1 + 6144, wbase + (unsigned)ch * 16384u, stg + q4 * 4096); }
        wait_vm<0>();
#pragma unroll
        for (int q4 = 0; q4 < 4; ++q4)
#pragma unroll
          for (int cc = 0; cc < 8; ++cc) wf[rnd * 4 + q4][cc] = tr_read(L + stg + q4 * 4096 + (ra ^ (unsigned)(cc << 5)));
        wait_lgkm();
      }
    }
    if (hw == 1 && (e16 % (32 / NFIN)) == 0) {
      const unsigned tl = lds_ld(L, C_TLO), th = lds_ld(L, C_THI);
      const unsigned tlo = tl << 16, thi = (tl >> 16) | (th << 16);
#pragma nounroll
      for (int bt = 0; bt < 8; ++bt) {
        v4u tv[8];
        for (unsigned s = 0; s < SPIN_MAX; ++s) {
          asm volatile("" ::: "memory");
          unsigned bad = 0u;
#pragma unroll
          for (int i = 0; i < 8; ++i) tv[i] = ld_sc1(rws, WS_TAB + (unsigned)(bt * 8 + i) * 1024u + lane * 16u);
#pragma unroll
          for (int i = 0; i < 8; ++i) bad |= (((tv[i].x ^ tlo) | (tv[i].z ^ tlo)) >> 16) | (tv[i].y ^ thi) | (tv[i].w ^ thi);
          if (__builtin_amdgcn_ballot_w64(bad != 0u) == 0ull) break;
          __builtin_amdgcn_s_sleep(64);
        }
#pragma unroll
        for (int i = 0; i < 8; ++i) *(LAS unsigned*)(L + L_STAGE + (bt * 8 + i) * 256 + lane * 4) = (tv[i].x & 0xffffu) | (tv[i].z << 16);
      }
      wait_lgkm();
      lds_st(L, C_TABOK, 1u);
    }
    if (hw == 3) {
      lds_wait(L, C_WO + 3, 1u);
      const u64 T48 = ((u64)lds_ld(L, C_TLO) | ((u64)lds_ld(L, C_THI) << 32)) << 16;
      const GAS u64* fp = (const GAS u64*)(p.ws + WS_Q) + b * 4096 + ((lane & 7) * 32 + 4 * b + ((lane >> 3) & 3)) * 16;
      for (unsigned s = 0; s < SPIN_MAX; ++s) {
        const u64 f = __hip_atomic_load(fp, __ATOMIC_RELAXED, __HIP_MEMORY_SCOPE_AGENT);
        if (__builtin_amdgcn_ballot_w64(((f ^ T48) >> 16) != 0ull) == 0ull) break;
        __builtin_amdgcn_s_sleep(64);
      }
      asm volatile("" ::: "memory");
      lds_st(L, C_FLAGOK, 1u);
    }
    lds_wait(L, C_FLAGOK, 1u);
    if (hw == 0) STAMP(6);
    lds_wait(L, C_MERGE, 2u);
    {
      const int sg = lane & 3, jq = (lane >> 2) & 3;
      const unsigned rec = WS_REC + (unsigned)(grp * 4 + sg) * REC_BYTES;
      const unsigned doff = rec + hw * 512 + (32 * jq + 4 * g) * 4;
      v4u d0, d1; v2u hd; unsigned ok = 0u;
      for (unsigned s = 0; s < SPIN_MAX; ++s) {
        asm volatile("" ::: "memory");
        if ((ok & 1u) == 0u) {
          hd = __builtin_amdgcn_raw_buffer_load_b64(rws, (int)(rec + 2048 + hw * 8), 0, 16);
          d0 = ld_sc1(rws, doff); d1 = ld_sc1(rws, doff + 64);
          ok = hd.x & hd.y & d0.x & d0.y & d0.z & d0.w & d1.x & d1.y & d1.z & d1.w;
        }
        if (__builtin_amdgcn_ballot_w64((ok & 1u) == 0u) == 0ull) break;
      }
      if (hw == 0) STAMP(11);
      const float mh = __uint_as_float(hd.x), lh = __uint_as_float(hd.y);
      float mx = fmaxf(mh, dpp<DPP_XOR1>(mh)); mx = fmaxf(mx, dpp<DPP_XOR2>(mx));
      const float e = __builtin_amdgcn_exp2f((mh - mx) * LOG2E);
      float lt = e * lh; lt += dpp<DPP_XOR1>(lt); lt += dpp<DPP_XOR2>(lt);
      const float w = e * __builtin_amdgcn_rcpf(lt);
      float ov[8];
#pragma unroll
      for (int i = 0; i < 4; ++i) { ov[i] = __uint_as_float(d0[i]) * w; ov[4 + i] = __uint_as_float(d1[i]) * w; }
#pragma unroll
      for (int i = 0; i < 8; ++i) { ov[i] += dpp<DPP_XOR1>(ov[i]); ov[i] += dpp<DPP_XOR2>(ov[i]); }
      const v4u af = {pk_bf16(ov[0], ov[1]), pk_bf16(ov[2], ov[3]), pk_bf16(ov[4], ov[5]), pk_bf16(ov[6], ov[7])};
      f32x4 acc[8];
#pragma unroll
      for (int cc = 0; cc < 8; ++cc) acc[cc] = f32x4{0.f, 0.f, 0.f, 0.f};
#pragma unroll
      for (int j = 0; j < 4; ++j) {
        const v4u aj = jq == j ? af : v4u{0u, 0u, 0u, 0u};
#pragma unroll
        for (int cc = 0; cc < 8; ++cc) acc[cc] = mfma32(aj, v4u{wf[2 * j][cc].x, wf[2 * j][cc].y, wf[2 * j + 1][cc].x, wf[2 * j + 1][cc].y}, acc[cc]);
      }
      *(LAS float*)(L + L_Y + hw * 512 + lane * 4) = xrow_sum4(acc[0][0], acc[1][0], acc[2][0], acc[3][0]);
      *(LAS float*)(L + L_Y + hw * 512 + 256 + lane * 4) = xrow_sum4(acc[4][0], acc[5][0], acc[6][0], acc[7][0]);
    }
    wait_lgkm();
    unsigned old = 0;
    if (lane == 0) old = __hip_atomic_fetch_add((LAS unsigned*)(L + L_CTL + 4 * C_YCNT), 1u, __ATOMIC_RELAXED, __HIP_MEMORY_SCOPE_WORKGROUP);
    old = __builtin_amdgcn_readfirstlane(old);
    asm volatile("" ::: "memory");
    if (old == 3u) {
      STAMP(7);
      if (lane < 32) {
        const f32x4 y0 = *(LAS f32x4*)(L + L_Y + lane * 16), y1 = *(LAS f32x4*)(L + L_Y + 512 + lane * 16);
        const f32x4 y2 = *(LAS f32x4*)(L + L_Y + 1024 + lane * 16), y3 = *(LAS f32x4*)(L + L_Y + 1536 + lane * 16);
        const f32x4 y = (y0 + y1) + (y2 + y3);
        st_sc1(rws, yslot + lane * 16, v4u{tagf(y[0]), tagf(y[1]), tagf(y[2]), tagf(y[3])});
      }
    }
  }
}

}

extern "C" void kernel_launch(void* const* d_in, const int* in_sizes, int n_in, void* d_out, int out_size, void* d_ws, size_t ws_size,
                              hipStream_t stream) {
  Params p;
  p.x = (const unsigned short*)d_in[0]; p.k = (const unsigned short*)d_in[1]; p.v = (const unsigned short*)d_in[2];
  p.wq = (const unsigned short*)d_in[3]; p.wo = (const unsigned short*)d_in[4];
  p.w1 = (const unsigned short*)d_in[6]; p.b1 = (const unsigned short*)d_in[7]; p.w2 = (const unsigned short*)d_in[8]; p.b2 = (const unsigned short*)d_in[9];
  p.out = (unsigned short*)d_out; p.ws = (unsigned char*)d_ws;
  (void)hipFuncSetAttribute((const void*)fused_decode_kernel, hipFuncAttributeMaxDynamicSharedMemorySize, (int)L_END + 128);
  hipLaunchKernelGGL(fused_decode_kernel, dim3(NWG), dim3(NTHR), L_END + 128, stream, p);
}
```

```cpp
#include <hip/hip_runtime.h>
#include <hip/hip_bf16.h>
#include <stdint.h>

#define LAS __attribute__((address_space(3)))
#define GAS __attribute__((address_space(1)))
typedef unsigned v4u __attribute__((ext_vector_type(4)));
typedef unsigned v2u __attribute__((ext_vector_type(2)));
typedef int v4i __attribute__((ext_vector_type(4)));
typedef float f32x4 __attribute__((ext_vector_type(4)));
typedef float f32x2 __attribute__((ext_vector_type(2)));
typedef short s16x4 __attribute__((ext_vector_type(4)));
typedef __bf16 bf16x8 __attribute__((ext_vector_type(8)));
typedef __bf16 bf16x2 __attribute__((ext_vector_type(2)));
typedef unsigned long long u64;

namespace {

constexpr int NB = 8, DM = 512, HQ = 32, HKV = 8, DH = 128;
constexpr int NWG = 256, NTHR = 512;
constexpr int TILES = 32;
constexpr int NFIN = 4;
constexpr float LN_EPS = 1e-5f;
constexpr float SCALE = 0.08838834764831845f, LOG2E = 1.4426950408889634f;

constexpr unsigned WS_EPOCH = 0;
constexpr unsigned WS_Y = 8192;
constexpr unsigned WS_Q = WS_Y + NB * HKV * DM * 4;
constexpr unsigned REC_BYTES = 2304;
constexpr unsigned WS_REC = WS_Q + NB * 4096 * 8;
constexpr unsigned WS_TAB = WS_REC + NWG * REC_BYTES;
constexpr unsigned WS_END = WS_TAB + 8192 * 8;

constexpr unsigned L_RING = 0;
constexpr unsigned L_STAGE = 131072;
constexpr unsigned L_QBUF = L_STAGE + 16384;
constexpr unsigned L_PART = L_QBUF + 1024;
constexpr unsigned L_XN = L_PART, XN_ROW = 1040;
constexpr unsigned L_ML = L_PART + 8320;
constexpr unsigned L_Y = L_ML + 128;
constexpr unsigned L_MLP = L_Y + 2048;
constexpr unsigned L_CTL = L_MLP + 512;
constexpr unsigned L_END = L_CTL + 128;
static_assert(L_END <= 160 * 1024, "LDS budget");
enum { C_QRDY = 0, C_XN = 1, C_MERGE = 2, C_YCNT = 3, C_TLO = 4, C_THI = 5, C_FLAGOK = 8, C_TCNT = 9, C_TABOK = 10, C_WO = 12   };

constexpr unsigned SPIN_MAX = 1u << 18;

struct Params {
  const unsigned short* k; const unsigned short* v; const unsigned short* wo; unsigned char* ws;
  const unsigned short* x; const unsigned short* wq;
  const unsigned short* w1; const unsigned short* b1; const unsigned short* w2; const unsigned short* b2;
  unsigned short* out;
};

__device__ __forceinline__ v4i mkrsrc(const void* p, unsigned bytes) {
  const u64 a = (u64)p;
  v4i r; r.x = __builtin_amdgcn_readfirstlane((int)(unsigned)a); r.y = __builtin_amdgcn_readfirstlane((int)((unsigned)(a >> 32) & 0xffffu));
  r.z = (int)bytes; r.w = 0x00020000; return r;
}
__device__ __forceinline__ void dma4_nt(v4i rs, unsigned v0, unsigned v1, unsigned v2, unsigned v3, unsigned soff, unsigned lds) {
  asm volatile("s_mov_b32 m0, %6\n\ts_nop 0\n\t"
               "buffer_load_dwordx4 %0, %4, %5 offen nt lds\n\t"
               "buffer_load_dwordx4 %1, %4, %5 offen offset:1024 nt lds\n\t"
               "buffer_load_dwordx4 %2, %4, %5 offen offset:2048 nt lds\n\t"
               "buffer_load_dwordx4 %3, %4, %5 offen offset:3072 nt lds"
               :: "v"(v0), "v"(v1), "v"(v2), "v"(v3), "s"(rs), "s"(soff), "s"(lds) : "memory");
}
__device__ __forceinline__ void dma4_w(v4i rs, unsigned v0, unsigned v1, unsigned v2, unsigned v3, unsigned soff, unsigned lds) {
  asm volatile("s_mov_b32 m0, %6\n\ts_nop 0\n\t"
               "buffer_load_dwordx4 %0, %4, %5 offen lds\n\t"
               "buffer_load_dwordx4 %1, %4, %5 offen offset:1024 lds\n\t"
               "buffer_load_dwordx4 %2, %4, %5 offen offset:2048 lds\n\t"
               "buffer_load_dwordx4 %3, %4, %5 offen offset:3072 lds"
               :: "v"(v0), "v"(v1), "v"(v2), "v"(v3), "s"(rs), "s"(soff), "s"(lds) : "memory");
}
template <int N> __device__ __forceinline__ void wait_vm() { asm volatile("s_waitcnt vmcnt(%0)" :: "i"(N) : "memory"); }
__device__ __forceinline__ void wait_lgkm() { asm volatile("s_waitcnt lgkmcnt(0)" ::: "memory"); }
__device__ __forceinline__ void wg_barrier() { asm volatile("s_waitcnt lgkmcnt(0)\n\ts_barrier" ::: "memory"); }

template <int CTRL> __device__ __forceinline__ float dpp(float x) {
  return __builtin_bit_cast(float, __builtin_amdgcn_mov_dpp(__builtin_bit_cast(int, x), CTRL, 0xf, 0xf, true));
}
constexpr int DPP_XOR1 = 0xB1, DPP_XOR2 = 0x4E, DPP_HMIRROR = 0x141, DPP_ROR8 = 0x128;
__device__ __forceinline__ float xrow_max(float x) {
  auto s = __builtin_amdgcn_permlane16_swap(__float_as_uint(x), __float_as_uint(x), false, false);
  x = fmaxf(__uint_as_float(s[0]), __uint_as_float(s[1]));
  auto t = __builtin_amdgcn_permlane32_swap(__float_as_uint(x), __float_as_uint(x), false, false);
  return fmaxf(__uint_as_float(t[0]), __uint_as_float(t[1]));
}
__device__ __forceinline__ float xrow_sum(float x) {
  auto s = __builtin_amdgcn_permlane16_swap(__float_as_uint(x), __float_as_uint(x), false, false);
  x = __uint_as_float(s[0]) + __uint_as_float(s[1]);
  auto t = __builtin_amdgcn_permlane32_swap(__float_as_uint(x), __float_as_uint(x), false, false);
  return __uint_as_float(t[0]) + __uint_as_float(t[1]);
}
__device__ __forceinline__ float row16_sum(float x) {
  x += dpp<DPP_XOR1>(x); x += dpp<DPP_XOR2>(x); x += dpp<DPP_HMIRROR>(x); x += dpp<DPP_ROR8>(x); return x;
}
__device__ __forceinline__ float wave_sum(float x) { return xrow_sum(row16_sum(x)); }
__device__ __forceinline__ float xrow_sum4(float a, float b, float c, float d) {
  auto s01 = __builtin_amdgcn_permlane16_swap(__float_as_uint(a), __float_as_uint(b), false, false);
  auto s23 = __builtin_amdgcn_permlane16_swap(__float_as_uint(c), __float_as_uint(d), false, false);
  const float x = __uint_as_float(s01[0]) + __uint_as_float(s01[1]), y = __uint_as_float(s23[0]) + __uint_as_float(s23[1]);
  auto t = __builtin_amdgcn_permlane32_swap(__float_as_uint(x), __float_as_uint(y), false, false);
  return __uint_as_float(t[0]) + __uint_as_float(t[1]);
}
__device__ __forceinline__ unsigned pk_bf16(float lo, float hi) {
  bf16x2 r; r[0] = (__bf16)lo; r[1] = (__bf16)hi; return __builtin_bit_cast(unsigned, r);
}
__device__ __forceinline__ float bf_lo(unsigned u) { return __uint_as_float(u << 16); }
__device__ __forceinline__ float bf_hi(unsigned u) { return __uint_as_float(u & 0xffff0000u); }
__device__ __forceinline__ float bf1(unsigned short u) { return __uint_as_float((unsigned)u << 16); }
__device__ __forceinline__ float rbf(float x) { return bf_lo(pk_bf16(x, x)); }
__device__ __forceinline__ f32x2 rbf2(f32x2 v) {
  unsigned u; asm("v_cvt_pk_bf16_f32 %0, %1, %2" : "=v"(u) : "v"(v.x), "v"(v.y));
  return f32x2{bf_lo(u), bf_hi(u)};
}
__device__ __forceinline__ f32x4 mfma32(v4u a, v4u b, f32x4 c) {
  return __builtin_amdgcn_mfma_f32_16x16x32_bf16(__builtin_bit_cast(bf16x8, a), __builtin_bit_cast(bf16x8, b), c, 0, 0, 0);
}
__device__ __forceinline__ f32x4 mfma16(v2u a, v2u b, f32x4 c) {
  return __builtin_amdgcn_mfma_f32_16x16x16bf16_1k(__builtin_bit_cast(s16x4, a), __builtin_bit_cast(s16x4, b), c, 0, 0, 0);
}
__device__ __forceinline__ v2u tr_read(LAS unsigned char* p) {
  return __builtin_bit_cast(v2u, __builtin_amdgcn_ds_read_tr16_b64_v4i16((LAS s16x4*)p));
}
__device__ __forceinline__ v4u ld_sc1(__amdgpu_buffer_rsrc_t rs, unsigned off) { return __builtin_amdgcn_raw_buffer_load_b128(rs, (int)off, 0, 16); }
__device__ __forceinline__ void st_sc1(__amdgpu_buffer_rsrc_t rs, unsigned off, v4u v) { __builtin_amdgcn_raw_buffer_store_b128(v, rs, (int)off, 0, 16); }
__device__ __forceinline__ unsigned lds_ld(LAS unsigned char* L, int idx) { return *(volatile LAS unsigned*)(L + L_CTL + 4 * idx); }
__device__ __forceinline__ void lds_st(LAS unsigned char* L, int idx, unsigned v) { *(volatile LAS unsigned*)(L + L_CTL + 4 * idx) = v; }
__device__ __forceinline__ void lds_wait(LAS unsigned char* L, int idx, unsigned want) {
  for (unsigned s = 0; s < SPIN_MAX; ++s) { if (lds_ld(L, idx) >= want) break; __builtin_amdgcn_s_sleep(1); }
  asm volatile("" ::: "memory");
}
__device__ __forceinline__ unsigned tagf(float f) { return __float_as_uint(f) | 1u; }

struct AttnState { float m, l; f32x4 o[8]; };

constexpr int slot_of(int n) { return n < 24 ? (n & 7) : (n & 3); }
struct StreamCtx {
  LAS unsigned char* L; unsigned ring; v4i rk, rv; unsigned vk[4], vv[2], ka[4], va[8]; unsigned tbase;
  template <int M> __device__ __forceinline__ void issue(int j) const {
    if constexpr ((M & 1) == 0) dma4_nt(rk, vk[0], vk[1], vk[2], vk[3], tbase + (unsigned)j * 65536u, ring + slot_of(M) * 4096);
    else dma4_nt(rv, vv[0], vv[1], vv[0], vv[1], tbase + (unsigned)j * 65536u, ring + slot_of(M) * 4096);
  }
  template <int N> __device__ __forceinline__ void step(int t) const {
    wait_vm<(N + 1 < 2 * TILES) ? 4 : 0>();
    if constexpr (N + 2 < 2 * TILES) issue<N + 2>(t + 1);
  }
};

__device__ __forceinline__ void softmax_pv(AttnState& st, const f32x4 sc, const v2u (&vf)[8], bool lds_pending) {
  const unsigned r01 = pk_bf16(sc[0], sc[1]), r23 = pk_bf16(sc[2], sc[3]);
  const unsigned u01 = pk_bf16(bf_lo(r01) * SCALE, bf_hi(r01) * SCALE), u23 = pk_bf16(bf_lo(r23) * SCALE, bf_hi(r23) * SCALE);
  const float s0 = bf_lo(u01), s1 = bf_hi(u01), s2 = bf_lo(u23), s3 = bf_hi(u23);
  const float tm = xrow_max(fmaxf(fmaxf(s0, s1), fmaxf(s2, s3)));
  if (__builtin_amdgcn_ballot_w64(tm > st.m) != 0ull) {
    const float mn = fmaxf(st.m, tm);
    const float al = __builtin_amdgcn_exp2f((st.m - mn) * LOG2E);
    st.m = mn; st.l *= al;
    const float a0 = dpp<0x00>(al), a1 = dpp<0x55>(al), a2 = dpp<0xAA>(al), a3 = dpp<0xFF>(al);
#pragma unroll
    for (int c = 0; c < 8; ++c) { st.o[c][0] *= a0; st.o[c][1] *= a1; st.o[c][2] *= a2; st.o[c][3] *= a3; }
  }
  const float p0 = __builtin_amdgcn_exp2f((s0 - st.m) * LOG2E), p1 = __builtin_amdgcn_exp2f((s1 - st.m) * LOG2E);
  const float p2 = __builtin_amdgcn_exp2f((s2 - st.m) * LOG2E), p3 = __builtin_amdgcn_exp2f((s3 - st.m) * LOG2E);
  st.l += (p0 + p1) + (p2 + p3);
  v2u pf; pf.x = pk_bf16(p0, p1); pf.y = pk_bf16(p2, p3);
  if (lds_pending) wait_lgkm();
#pragma unroll
  for (int c = 0; c < 8; ++c) st.o[c] = mfma16(pf, vf[c], st.o[c]);
}
struct TileRegs { v4u kf[4]; v2u vf[8]; };
template <int TC> __device__ __forceinline__ void read_k(TileRegs& tr, const StreamCtx& cx) {
#pragma unroll
  for (int s = 0; s < 4; ++s) tr.kf[s] = *(LAS v4u*)(cx.L + cx.ring + slot_of(2 * TC) * 4096 + cx.ka[s]);
}
template <int TC> __device__ __forceinline__ void read_v(TileRegs& tr, const StreamCtx& cx) {
#pragma unroll
  for (int c = 0; c < 8; ++c) tr.vf[c] = tr_read(cx.L + cx.ring + slot_of(2 * TC) * 4096 + 4096 + cx.va[c]);
}
__device__ __forceinline__ f32x4 qk(const TileRegs& tr, const v4u (&qf)[4]) {
  f32x4 sc = {0.f, 0.f, 0.f, 0.f};
#pragma unroll
  for (int s = 0; s < 4; ++s) sc = mfma32(tr.kf[s], qf[s], sc);
  return sc;
}
template <int TC> __device__ __forceinline__ void attn_tile(AttnState& st, const StreamCtx& cx, const v4u (&qf)[4], int t) {
  TileRegs tr;
  cx.step<2 * TC>(t); read_k<TC>(tr, cx);
  const f32x4 sc = qk(tr, qf);
  cx.step<2 * TC + 1>(t); read_v<TC>(tr, cx);
  softmax_pv(st, sc, tr.vf, true);
}
template <int TC> __device__ __forceinline__ void ring_tile(AttnState& st, const StreamCtx& cx, const v4u (&qf)[4]) {
  TileRegs tr; read_k<TC>(tr, cx); read_v<TC>(tr, cx);
  softmax_pv(st, qk(tr, qf), tr.vf, true);
}
template <int TC> __device__ __forceinline__ void park_tile(TileRegs& tr, const StreamCtx& cx) {
  cx.step<2 * TC>(TC); read_k<TC>(tr, cx);
  cx.step<2 * TC + 1>(TC); read_v<TC>(tr, cx);
  wait_lgkm();
}
__device__ __forceinline__ void parked_tile(AttnState& st, const TileRegs& tr, const v4u (&qf)[4]) { softmax_pv(st, qk(tr, qf), tr.vf, false); }

__device__ __forceinline__ void ln_stats(const f32x2 (&z)[4], float& mb, float& rs) {
  const float pv = __builtin_bit_cast(float, __builtin_amdgcn_readfirstlane(__builtin_bit_cast(int, z[0].x)));
  f32x2 s = {0.f, 0.f}, q = {0.f, 0.f};
#pragma unroll
  for (int e = 0; e < 4; ++e) { const f32x2 d = z[e] - f32x2{pv, pv}; s += d; q = __builtin_elementwise_fma(d, d, q); }
  float s1 = s.x + s.y, q1 = q.x + q.y;
  s1 += dpp<DPP_XOR1>(s1); q1 += dpp<DPP_XOR1>(q1); s1 += dpp<DPP_XOR2>(s1); q1 += dpp<DPP_XOR2>(q1);
  s1 += dpp<DPP_HMIRROR>(s1); q1 += dpp<DPP_HMIRROR>(q1); s1 += dpp<DPP_ROR8>(s1); q1 += dpp<DPP_ROR8>(q1);
  const float dm = xrow_sum(s1) * (1.f / DM), q2 = xrow_sum(q1) * (1.f / DM);
  const float var = fmaxf(fmaf(-dm, dm, q2), 0.f);
  mb = rbf(pv + dm);
  rs = __builtin_amdgcn_rcpf(rbf(__builtin_amdgcn_sqrtf(rbf(rbf(var) + LN_EPS))));
}
__device__ __forceinline__ f32x2 ln_apply(f32x2 z, float mb, float rs) { return rbf2(rbf2(z - f32x2{mb, mb}) * f32x2{rs, rs}); }

constexpr float GELU_K0 = -2.f * 0.7978845608028654f * LOG2E, GELU_K1 = GELU_K0 * 0.044715f;
struct MlpCoef { float k3, k2, k1, k0, wa, wb; };
__device__ __forceinline__ MlpCoef mlp_coef(unsigned short w1r, unsigned short b1r, unsigned short w2r) {
  constexpr float K0 = GELU_K0, K1 = GELU_K1;
  const float a = bf1(w1r), bb = bf1(b1r), w2 = bf1(w2r);
  MlpCoef c;
  c.k3 = K1 * a * a * a; c.k2 = 3.f * K1 * a * a * bb; c.k1 = a * fmaf(3.f * K1 * bb, bb, K0); c.k0 = bb * fmaf(K1 * bb, bb, K0);
  c.wa = w2 * a; c.wb = w2 * bb;
  return c;
}
__device__ __forceinline__ f32x2 mlp_term(const MlpCoef& c, f32x2 t, f32x2 acc) {
  f32x2 ar = __builtin_elementwise_fma(f32x2{c.k3, c.k3}, t, f32x2{c.k2, c.k2});
  ar = __builtin_elementwise_fma(ar, t, f32x2{c.k1, c.k1});
  ar = __builtin_elementwise_fma(ar, t, f32x2{c.k0, c.k0});
  const f32x2 wu = __builtin_elementwise_fma(f32x2{c.wa, c.wa}, t, f32x2{c.wb, c.wb});
  f32x2 d = {__builtin_amdgcn_exp2f(ar.x), __builtin_amdgcn_exp2f(ar.y)};
  d += f32x2{1.f, 1.f};
  const f32x2 r = {__builtin_amdgcn_rcpf(d.x), __builtin_amdgcn_rcpf(d.y)};
  return __builtin_elementwise_fma(wu, r, acc);
}
constexpr unsigned TAB_LO = 100u << 7, TAB_N = 4096u, TAB_ZERO = TAB_N - 1u;

__global__ void __launch_bounds__(NTHR, 2) fused_decode_kernel(Params p) {
  extern __shared__ __attribute__((aligned(16))) unsigned char lds_raw[];
  LAS unsigned char* L = (LAS unsigned char*)lds_raw;
  const int tid = threadIdx.x, lane = tid & 63, wave = __builtin_amdgcn_readfirstlane(tid >> 6);
  const int c = blockIdx.x;
  asm volatile("" :: "s"(p.k), "s"(p.v), "s"(p.ws), "s"(p.x), "s"(p.wq), "s"(p.out));
  const int b = c >> 5, kvh = c & 7, sp = (c >> 3) & 3, grp = b * 8 + kvh;
  const int rid = grp * 4 + sp;
  const int cq = (c & 7) * 32 + (c >> 3);
  const int e16 = c & 31;
  const int g = lane >> 4, c16 = lane & 15;
  const __amdgpu_buffer_rsrc_t rws = __builtin_amdgcn_make_buffer_rsrc(p.ws, 0, WS_END, 0x00020000);

  const unsigned ra = 256 * (4 * g + (c16 >> 2)) + 16 * (((lane >> 1) & 1) ^ (((4 * g + (c16 >> 2)) & 7) << 1)) + 8 * (lane & 1);

  if (wave < 4) {
    __builtin_amdgcn_s_setprio(2);
    StreamCtx cx;
    cx.L = L; cx.rk = mkrsrc(p.k, 1u << 27); cx.rv = mkrsrc(p.v, 1u << 27);
#pragma unroll
    for (int i = 0; i < 4; ++i) cx.vk[i] = g * 256 + 16 * (c16 ^ (4 * i + g));
#pragma unroll
    for (int i = 0; i < 2; ++i) cx.vv[i] = g * 256 + 16 * (c16 ^ (((4 * i + g) & 7) << 1));
    cx.ring = L_RING + wave * 32768;
    cx.tbase = (unsigned)grp * (2048u * 1024u) + (unsigned)(sp * 4 + wave) * 4096u;
    cx.issue<0>(0); cx.issue<1>(0);
    wg_barrier();
#pragma unroll
    for (int s = 0; s < 4; ++s) cx.ka[s] = 256 * c16 + 16 * ((4 * s + g) ^ c16);
#pragma unroll
    for (int cc = 0; cc < 8; ++cc) cx.va[cc] = ra ^ (unsigned)(cc << 5);
    TileRegs pk0, pk1, pk2, pk3;
    park_tile<0>(pk0, cx); park_tile<1>(pk1, cx); park_tile<2>(pk2, cx); park_tile<3>(pk3, cx);
    cx.step<8>(4); cx.step<9>(4); cx.step<10>(5);
    lds_wait(L, C_QRDY, 1u);
    v4u qf[4];
#pragma unroll
    for (int s = 0; s < 4; ++s) qf[s] = *(LAS v4u*)(L + L_QBUF + (lane & 3) * 256 + s * 64 + g * 16);
    AttnState st; st.m = -INFINITY; st.l = 0.f;
#pragma unroll
    for (int cc = 0; cc < 8; ++cc) st.o[cc] = f32x4{0.f, 0.f, 0.f, 0.f};
    parked_tile(st, pk0, qf);
    cx.step<11>(5); parked_tile(st, pk1, qf);
    cx.step<12>(6); parked_tile(st, pk2, qf);
    cx.step<13>(6); parked_tile(st, pk3, qf);
    ring_tile<4>(st, cx, qf); cx.step<14>(7);
    cx.step<15>(7); ring_tile<5>(st, cx, qf);
    cx.step<16>(8); ring_tile<6>(st, cx, qf);
    cx.step<17>(8); ring_tile<7>(st, cx, qf);
    cx.step<18>(9); ring_tile<8>(st, cx, qf);
    cx.step<19>(9); ring_tile<9>(st, cx, qf);
    attn_tile<10>(st, cx, qf, 10); attn_tile<11>(st, cx, qf, 11);
    lds_st(L, C_WO + wave, 1u);
    for (int j = 12; j < TILES - 2; j += 2) { attn_tile<12>(st, cx, qf, j); attn_tile<13>(st, cx, qf, j + 1); }
    attn_tile<30>(st, cx, qf, 30); attn_tile<31>(st, cx, qf, 31);
    const float lsum = xrow_sum(st.l);
#pragma unroll
    for (int cc = 0; cc < 8; ++cc) {
      const float v = g == 0 ? st.o[cc][0] : g == 1 ? st.o[cc][1] : g == 2 ? st.o[cc][2] : st.o[cc][3];
      *(LAS float*)(L + L_PART + wave * 2048 + g * 512 + (16 * cc + c16) * 4) = v;
    }
    if (lane < 4) *(LAS v2u*)(L + L_ML + wave * 32 + lane * 8) = v2u{__float_as_uint(st.m), __float_as_uint(lsum)};
    wait_lgkm();
    unsigned old = 0;
    if (lane == 0) old = __hip_atomic_fetch_add((LAS unsigned*)(L + L_CTL + 4 * C_MERGE), 1u, __ATOMIC_RELAXED, __HIP_MEMORY_SCOPE_WORKGROUP);
    old = __builtin_amdgcn_readfirstlane(old);
    asm volatile("" ::: "memory");
    if (old == 3u) {
      float mw[4], lw[4];
#pragma unroll
      for (int w = 0; w < 4; ++w) { const v2u ml = *(LAS v2u*)(L + L_ML + w * 32 + g * 8); mw[w] = __uint_as_float(ml.x); lw[w] = __uint_as_float(ml.y); }
      const float M = fmaxf(fmaxf(mw[0], mw[1]), fmaxf(mw[2], mw[3]));
      float Ls = 0.f; f32x4 a0 = {0.f, 0.f, 0.f, 0.f}, a1 = {0.f, 0.f, 0.f, 0.f};
#pragma unroll
      for (int w = 0; w < 4; ++w) {
        const float f = __builtin_amdgcn_exp2f((mw[w] - M) * LOG2E);
        const f32x4 x0 = *(LAS f32x4*)(L + L_PART + w * 2048 + g * 512 + c16 * 16), x1 = *(LAS f32x4*)(L + L_PART + w * 2048 + g * 512 + 256 + c16 * 16);
        Ls = fmaf(f, lw[w], Ls); a0 += f * x0; a1 += f * x1;
      }
      const unsigned rec = WS_REC + (unsigned)rid * REC_BYTES;
      st_sc1(rws, rec + g * 512 + c16 * 16, v4u{tagf(a0[0]), tagf(a0[1]), tagf(a0[2]), tagf(a0[3])});
      st_sc1(rws, rec + g * 512 + 256 + c16 * 16, v4u{tagf(a1[0]), tagf(a1[1]), tagf(a1[2]), tagf(a1[3])});
      if (c16 == 0) __builtin_amdgcn_raw_buffer_store_b64(v2u{tagf(M), tagf(Ls)}, rws, (int)(rec + 2048 + g * 8), 0, 16);
    }
    if (wave == 0 && (e16 % (32 / NFIN)) == 0) {
      const unsigned short b2r = p.b2[0];
      const v2u xr0 = *(const GAS v2u*)(p.x + b * DM + lane * 4), xr1 = *(const GAS v2u*)(p.x + b * DM + 256 + lane * 4);
      lds_wait(L, C_FLAGOK, 1u);
      lds_wait(L, C_TABOK, 1u);
      const unsigned tl = lds_ld(L, C_TLO), th = lds_ld(L, C_THI);
      const unsigned yoff = WS_Y + (unsigned)b * 16384u + lane * 16;
      v4u yv[16]; unsigned pend = 0xffffu;
      for (unsigned s = 0; s < SPIN_MAX; ++s) {
        asm volatile("" ::: "memory");
#pragma unroll
        for (int i = 0; i < 16; ++i) if ((pend >> i) & 1u) yv[i] = ld_sc1(rws, yoff + i * 1024);
#pragma unroll
        for (int i = 0; i < 16; ++i) if ((pend >> i) & 1u) {
          const unsigned ok = yv[i].x & yv[i].y & yv[i].z & yv[i].w;
          if (__builtin_amdgcn_ballot_w64((ok & 1u) == 0u) == 0ull) pend &= ~(1u << i);
        }
        if (pend == 0u) break;
        if (__builtin_popcount(pend) > 4) __builtin_amdgcn_s_sleep(1);
      }
      f32x2 hv[4], z[4];
#pragma unroll
      for (int hf = 0; hf < 2; ++hf) {
        f32x4 ys[8];
#pragma unroll
        for (int kk = 0; kk < 8; ++kk) ys[kk] = __builtin_bit_cast(f32x4, yv[2 * kk + hf]);
        const f32x4 y = ((ys[0] + ys[1]) + (ys[2] + ys[3])) + ((ys[4] + ys[5]) + (ys[6] + ys[7]));
        const v2u xr = hf ? xr1 : xr0;
        hv[2 * hf] = rbf2(f32x2{bf_lo(xr.x), bf_hi(xr.x)} + rbf2(f32x2{y[0], y[1]}));
        hv[2 * hf + 1] = rbf2(f32x2{bf_lo(xr.y), bf_hi(xr.y)} + rbf2(f32x2{y[2], y[3]}));
      }
      float mb, rs;
      ln_stats(hv, mb, rs);
#pragma unroll
      for (int e = 0; e < 4; ++e) z[e] = ln_apply(hv[e], mb, rs);
      ln_stats(z, mb, rs);
      const float b2f = bf1(b2r);
#pragma unroll
      for (int hf = 0; hf < 2; ++hf) {
        if (NFIN > 1 && (e16 >> 4) != hf) continue;
        constexpr int NL = NFIN >= 2 ? 128 / NFIN : 64;
        const int l0 = NFIN >= 2 ? (4 * e16) & 63 : 0;
        const bool mine = lane >= l0 && lane < l0 + NL;
        const f32x2 za = ln_apply(z[2 * hf], mb, rs), zb = ln_apply(z[2 * hf + 1], mb, rs);
        const float tz[4] = {za.x, za.y, zb.x, zb.y}, h4[4] = {hv[2 * hf].x, hv[2 * hf].y, hv[2 * hf + 1].x, hv[2 * hf + 1].y};
        unsigned idx[4]; bool oot[4];
#pragma unroll
        for (int e = 0; e < 4; ++e) {
          const unsigned bits = __float_as_uint(tz[e]) >> 16, mag = bits & 0x7fffu;
          const unsigned rel = mag == 0u ? TAB_ZERO : mag - TAB_LO;
          const bool outside = mag != 0u && rel >= TAB_ZERO;
          oot[e] = mine && outside;
          idx[e] = outside ? 0u : rel * 2u + (bits >> 15);
        }
        float y0 = bf1(*(LAS unsigned short*)(L + L_STAGE + idx[0] * 2u)), y1 = bf1(*(LAS unsigned short*)(L + L_STAGE + idx[1] * 2u));
        float y2 = bf1(*(LAS unsigned short*)(L + L_STAGE + idx[2] * 2u)), y3 = bf1(*(LAS unsigned short*)(L + L_STAGE + idx[3] * 2u));
        if (__builtin_amdgcn_ballot_w64(oot[0] || oot[1] || oot[2] || oot[3]) != 0ull) {
#pragma nounroll
          for (int e = 0; e < 4; ++e) {
            const float te = e == 0 ? tz[0] : e == 1 ? tz[1] : e == 2 ? tz[2] : tz[3];
            u64 todo = __builtin_amdgcn_ballot_w64(e == 0 ? oot[0] : e == 1 ? oot[1] : e == 2 ? oot[2] : oot[3]);
            while (todo != 0ull) {
              const int src = __builtin_ctzll(todo); todo &= todo - 1ull;
              const float tv = __builtin_bit_cast(float, __builtin_amdgcn_readlane(__builtin_bit_cast(int, te), src));
              float acc = 0.f;
#pragma nounroll
              for (int i = 0; i < 28; ++i) {
                const float u = fmaf(tv, bf1(p.w1[lane + 64 * i]), bf1(p.b1[lane + 64 * i]));
                const float ex = __builtin_amdgcn_exp2f(u * fmaf(u * u, GELU_K1, GELU_K0));
                acc = fmaf(u * bf1(p.w2[lane + 64 * i]), __builtin_amdgcn_rcpf(1.f + ex), acc);
              }
              const float yd = rbf(wave_sum(acc));
              if (lane == src) { if (e == 0) y0 = yd; else if (e == 1) y1 = yd; else if (e == 2) y2 = yd; else y3 = yd; }
            }
          }
        }
        if (mine) {
          const v2u o = {pk_bf16(h4[0] + rbf(y0 + b2f), h4[1] + rbf(y1 + b2f)), pk_bf16(h4[2] + rbf(y2 + b2f), h4[3] + rbf(y3 + b2f))};
          *(GAS v2u*)(p.out + b * DM + hf * 256 + lane * 4) = o;
        }
      }
      if (c == 0 && lane == 0) __hip_atomic_store((GAS u64*)(p.ws + WS_EPOCH), (u64)tl | ((u64)th << 32), __ATOMIC_RELAXED, __HIP_MEMORY_SCOPE_AGENT);
    }
  } else {
    const int hw = wave - 4;
    const unsigned yslot = WS_Y + (((unsigned)b * 8u + (unsigned)kvh) * 512u + (unsigned)sp * 128u) * 4u;
    if (hw == 0) {
      const v4i rq = mkrsrc(p.wq, DM * HQ * DH * 2);
      const unsigned vo = (lane >> 1) * 8192 + (lane & 1) * 16;
#pragma unroll
      for (int s4 = 0; s4 < 4; ++s4)
        dma4_w(rq, vo, vo + 32u * 8192u - 1024u, vo + 64u * 8192u - 2048u, vo + 96u * 8192u - 3072u, (unsigned)cq * 32u + (unsigned)s4 * (128u * 8192u), L_STAGE + s4 * 4096);
      const u64 ep = __hip_atomic_load((GAS u64*)(p.ws + WS_EPOCH), __ATOMIC_RELAXED, __HIP_MEMORY_SCOPE_AGENT);
      wg_barrier();
      const u64 T = ep + 0x9E3779B97F4A7C15ull;
      if (lane == 0) { lds_st(L, C_TLO, (unsigned)T); lds_st(L, C_THI, (unsigned)(T >> 32)); }
      wait_vm<0>();
      lds_wait(L, C_XN, 3u);
      f32x4 d = {0.f, 0.f, 0.f, 0.f};
      const unsigned ta = (8 * g + (c16 >> 2)) * 32 + (lane & 3) * 8;
#pragma unroll
      for (int s = 0; s < 16; ++s) {
        const v2u lo = tr_read(L + L_STAGE + s * 1024 + ta), hi = tr_read(L + L_STAGE + s * 1024 + ta + 128);
        const v4u xf = *(LAS v4u*)(L + L_XN + (c16 & 7) * XN_ROW + s * 64 + g * 16);
        d = mfma32(v4u{lo.x, lo.y, hi.x, hi.y}, xf, d);
      }
      const unsigned p0 = pk_bf16(d[0], d[1]), p1 = pk_bf16(d[2], d[3]);
      const int src = (c16 + 16 * (2 * (c16 >> 3) + (g >> 1))) * 4;
      const unsigned q0 = (unsigned)__builtin_amdgcn_ds_bpermute(src, (int)p0), q1 = (unsigned)__builtin_amdgcn_ds_bpermute(src, (int)p1);
      const unsigned qp = (g & 1) ? q1 : q0;
      const unsigned tlo = (unsigned)T << 16, thi = (unsigned)(T >> 16);
      st_sc1(rws, WS_Q + ((unsigned)(c16 & 7) * 4096u + (unsigned)cq * 16u) * 8u + (4 * (c16 >> 3) + g) * 16u,
             v4u{(qp & 0xffffu) | tlo, thi, (qp >> 16) | tlo, thi});
      const unsigned qoff = WS_Q + ((unsigned)b * 4096u + (unsigned)kvh * 512u) * 8u + lane * 16u;
      v4u a0, a1, a2, a3; unsigned bad = 1u;
      __builtin_amdgcn_s_sleep(64);
      for (unsigned s = 0; s < SPIN_MAX; ++s) {
        asm volatile("" ::: "memory");
        if (bad != 0u) {
          a0 = ld_sc1(rws, qoff); a1 = ld_sc1(rws, qoff + 1024); a2 = ld_sc1(rws, qoff + 2048); a3 = ld_sc1(rws, qoff + 3072);
          bad = (((a0.x ^ tlo) | (a0.z ^ tlo) | (a1.x ^ tlo) | (a1.z ^ tlo) | (a2.x ^ tlo) | (a2.z ^ tlo) | (a3.x ^ tlo) | (a3.z ^ tlo)) >> 16) |
                (a0.y ^ thi) | (a0.w ^ thi) | (a1.y ^ thi) | (a1.w ^ thi) | (a2.y ^ thi) | (a2.w ^ thi) | (a3.y ^ thi) | (a3.w ^ thi);
        }
        if (__builtin_amdgcn_ballot_w64(bad != 0u) == 0ull) break;
        __builtin_amdgcn_s_sleep(32);
      }
      *(LAS unsigned*)(L + L_QBUF + lane * 4) = (a0.x & 0xffffu) | (a0.z << 16);
      *(LAS unsigned*)(L + L_QBUF + 256 + lane * 4) = (a1.x & 0xffffu) | (a1.z << 16);
      *(LAS unsigned*)(L + L_QBUF + 512 + lane * 4) = (a2.x & 0xffffu) | (a2.z << 16);
      *(LAS unsigned*)(L + L_QBUF + 768 + lane * 4) = (a3.x & 0xffffu) | (a3.z << 16);
      wait_lgkm();
      lds_st(L, C_QRDY, 1u);
    } else if (hw != 3) {
      v4u xv[4];
#pragma unroll
      for (int i = 0; i < 4; ++i) xv[i] = *((const GAS v4u*)(p.x + ((hw - 1) * 4 + i) * DM) + lane);
      wg_barrier();
#pragma unroll
      for (int i = 0; i < 4; ++i) {
        f32x2 z[4]; float mb, rs; v4u o;
#pragma unroll
        for (int e = 0; e < 4; ++e) z[e] = f32x2{bf_lo(xv[i][e]), bf_hi(xv[i][e])};
        ln_stats(z, mb, rs);
#pragma unroll
        for (int e = 0; e < 4; ++e) { const f32x2 t = rbf2(z[e] - f32x2{mb, mb}) * f32x2{rs, rs}; o[e] = pk_bf16(t.x, t.y); }
        *(LAS v4u*)(L + L_XN + ((hw - 1) * 4 + i) * XN_ROW + lane * 16) = o;
      }
      wait_lgkm();
      if (lane == 0) __hip_atomic_fetch_add((LAS unsigned*)(L + L_CTL + 4 * C_XN), 1u, __ATOMIC_RELAXED, __HIP_MEMORY_SCOPE_WORKGROUP);
    } else {
      const v4u z = {0u, 0u, 0u, 0u};
      const unsigned rec = WS_REC + (unsigned)rid * REC_BYTES;
      st_sc1(rws, rec + lane * 16, z); st_sc1(rws, rec + 1024 + lane * 16, z);
      if (lane < 2) st_sc1(rws, rec + 2048 + lane * 16, z);
      if (lane < 32) st_sc1(rws, yslot + lane * 16, z);
      if (lane < 32) lds_st(L, lane, 0u);
      wg_barrier();
      wait_vm<0>();
      if (lane == 0) __hip_atomic_fetch_add((LAS unsigned*)(L + L_CTL + 4 * C_XN), 1u, __ATOMIC_RELAXED, __HIP_MEMORY_SCOPE_WORKGROUP);
    }
    {
      MlpCoef cf[7];
      const int f0 = tid & 255;
#pragma unroll
      for (int i = 0; i < 7; ++i) cf[i] = mlp_coef(p.w1[f0 + 256 * i], p.b1[f0 + 256 * i], p.w2[f0 + 256 * i]);
#pragma nounroll
      for (int ch = 0; ch < 4; ++ch) {
        f32x2 acc[4], tp[4];
#pragma unroll
        for (int e = 0; e < 4; ++e) {
          const unsigned mag = (unsigned)(c * 16 + ch * 4 + e);
          const float tv = mag == TAB_ZERO ? 0.f : __uint_as_float((TAB_LO + mag) << 16);
          tp[e] = f32x2{tv, -tv}; acc[e] = f32x2{0.f, 0.f};
        }
#pragma unroll
        for (int i = 0; i < 7; ++i)
#pragma unroll
          for (int e = 0; e < 4; ++e) acc[e] = mlp_term(cf[i], tp[e], acc[e]);
        const float r0 = row16_sum(xrow_sum4(acc[0].x, acc[0].y, acc[1].x, acc[1].y));
        const float r1 = row16_sum(xrow_sum4(acc[2].x, acc[2].y, acc[3].x, acc[3].y));
        if (c16 == 0) { *(LAS float*)(L + L_MLP + hw * 128 + ch * 32 + g * 4) = r0; *(LAS float*)(L + L_MLP + hw * 128 + ch * 32 + 16 + g * 4) = r1; }
      }
      wait_lgkm();
      unsigned old = 0;
      if (lane == 0) old = __hip_atomic_fetch_add((LAS unsigned*)(L + L_CTL + 4 * C_TCNT), 1u, __ATOMIC_RELAXED, __HIP_MEMORY_SCOPE_WORKGROUP);
      old = __builtin_amdgcn_readfirstlane(old);
      asm volatile("" ::: "memory");
      if (old == 3u && lane < 16) {
        f32x2 y = {0.f, 0.f};
#pragma unroll
        for (int w = 0; w < 4; ++w) y += *(LAS f32x2*)(L + L_MLP + w * 128 + lane * 8);
        const unsigned yp = pk_bf16(y.x, y.y), tl = lds_ld(L, C_TLO), th = lds_ld(L, C_THI);
        const unsigned tlo = tl << 16, thi = (tl >> 16) | (th << 16);
        st_sc1(rws, WS_TAB + (unsigned)c * 256u + lane * 16u, v4u{(yp & 0xffffu) | tlo, thi, (yp >> 16) | tlo, thi});
      }
    }
    lds_wait(L, C_WO + hw, 1u);
    v2u wf[8][8];
    {
      const v4i ro = mkrsrc(p.wo, HQ * DH * DM * 2);
      const unsigned stg = L_RING + hw * 32768 + 16384;
      const unsigned wbase = (unsigned)(kvh * 512 + hw * 128) * 1024u + (unsigned)sp * 256u;
      const unsigned w0 = g * 1024 + 16 * (c16 ^ (g << 1)), w1 = g * 1024 + 3072 + 16 * (c16 ^ ((4 + g) << 1));
#pragma unroll
      for (int rnd = 0; rnd < 2; ++rnd) {
#pragma unroll
        for (int q4 = 0; q4 < 4; ++q4) { const int ch = rnd * 4 + q4; dma4_w(ro, w0, w1, w0 + 6144, w1 + 6144, wbase + (unsigned)ch * 16384u, stg + q4 * 4096); }
        wait_vm<0>();
#pragma unroll
        for (int q4 = 0; q4 < 4; ++q4)
#pragma unroll
          for (int cc = 0; cc < 8; ++cc) wf[rnd * 4 + q4][cc] = tr_read(L + stg + q4 * 4096 + (ra ^ (unsigned)(cc << 5)));
        wait_lgkm();
      }
    }
    if (hw == 1 && (e16 % (32 / NFIN)) == 0) {
      const unsigned tl = lds_ld(L, C_TLO), th = lds_ld(L, C_THI);
      const unsigned tlo = tl << 16, thi = (tl >> 16) | (th << 16);
#pragma nounroll
      for (int bt = 0; bt < 8; ++bt) {
        v4u tv[8];
        for (unsigned s = 0; s < SPIN_MAX; ++s) {
          asm volatile("" ::: "memory");
          unsigned bad = 0u;
#pragma unroll
          for (int i = 0; i < 8; ++i) tv[i] = ld_sc1(rws, WS_TAB + (unsigned)(bt * 8 + i) * 1024u + lane * 16u);
#pragma unroll
          for (int i = 0; i < 8; ++i) bad |= (((tv[i].x ^ tlo) | (tv[i].z ^ tlo)) >> 16) | (tv[i].y ^ thi) | (tv[i].w ^ thi);
          if (__builtin_amdgcn_ballot_w64(bad != 0u) == 0ull) break;
          __builtin_amdgcn_s_sleep(64);
        }
#pragma unroll
        for (int i = 0; i < 8; ++i) *(LAS unsigned*)(L + L_STAGE + (bt * 8 + i) * 256 + lane * 4) = (tv[i].x & 0xffffu) | (tv[i].z << 16);
      }
      wait_lgkm();
      lds_st(L, C_TABOK, 1u);
    }
    if (hw == 3) {
      lds_wait(L, C_WO + 3, 1u);
      const u64 T48 = ((u64)lds_ld(L, C_TLO) | ((u64)lds_ld(L, C_THI) << 32)) << 16;
      const GAS u64* fp = (const GAS u64*)(p.ws + WS_Q) + b * 4096 + ((lane & 7) * 32 + 4 * b + ((lane >> 3) & 3)) * 16;
      for (unsigned s = 0; s < SPIN_MAX; ++s) {
        const u64 f = __hip_atomic_load(fp, __ATOMIC_RELAXED, __HIP_MEMORY_SCOPE_AGENT);
        if (__builtin_amdgcn_ballot_w64(((f ^ T48) >> 16) != 0ull) == 0ull) break;
        __builtin_amdgcn_s_sleep(64);
      }
      asm volatile("" ::: "memory");
      lds_st(L, C_FLAGOK, 1u);
    }
    lds_wait(L, C_FLAGOK, 1u);
    lds_wait(L, C_MERGE, 2u);
    {
      const int sg = lane & 3, jq = (lane >> 2) & 3;
      const unsigned rec = WS_REC + (unsigned)(grp * 4 + sg) * REC_BYTES;
      const unsigned doff = rec + hw * 512 + (32 * jq + 4 * g) * 4;
      v4u d0, d1; v2u hd; unsigned ok = 0u;
      for (unsigned s = 0; s < SPIN_MAX; ++s) {
        asm volatile("" ::: "memory");
        if ((ok & 1u) == 0u) {
          hd = __builtin_amdgcn_raw_buffer_load_b64(rws, (int)(rec + 2048 + hw * 8), 0, 16);
          d0 = ld_sc1(rws, doff); d1 = ld_sc1(rws, doff + 64);
          ok = hd.x & hd.y & d0.x & d0.y & d0.z & d0.w & d1.x & d1.y & d1.z & d1.w;
        }
        if (__builtin_amdgcn_ballot_w64((ok & 1u) == 0u) == 0ull) break;
      }
      const float mh = __uint_as_float(hd.x), lh = __uint_as_float(hd.y);
      float mx = fmaxf(mh, dpp<DPP_XOR1>(mh)); mx = fmaxf(mx, dpp<DPP_XOR2>(mx));
      const float e = __builtin_amdgcn_exp2f((mh - mx) * LOG2E);
      float lt = e * lh; lt += dpp<DPP_XOR1>(lt); lt += dpp<DPP_XOR2>(lt);
      const float w = e * __builtin_amdgcn_rcpf(lt);
      float ov[8];
#pragma unroll
      for (int i = 0; i < 4; ++i) { ov[i] = __uint_as_float(d0[i]) * w; ov[4 + i] = __uint_as_float(d1[i]) * w; }
#pragma unroll
      for (int i = 0; i < 8; ++i) { ov[i] += dpp<DPP_XOR1>(ov[i]); ov[i] += dpp<DPP_XOR2>(ov[i]); }
      const v4u af = {pk_bf16(ov[0], ov[1]), pk_bf16(ov[2], ov[3]), pk_bf16(ov[4], ov[5]), pk_bf16(ov[6], ov[7])};
      f32x4 acc[8];
#pragma unroll
      for (int cc = 0; cc < 8; ++cc) acc[cc] = f32x4{0.f, 0.f, 0.f, 0.f};
#pragma unroll
      for (int j = 0; j < 4; ++j) {
        const v4u aj = jq == j ? af : v4u{0u, 0u, 0u, 0u};
#pragma unroll
        for (int cc = 0; cc < 8; ++cc) acc[cc] = mfma32(aj, v4u{wf[2 * j][cc].x, wf[2 * j][cc].y, wf[2 * j + 1][cc].x, wf[2 * j + 1][cc].y}, acc[cc]);
      }
      *(LAS float*)(L + L_Y + hw * 512 + lane * 4) = xrow_sum4(acc[0][0], acc[1][0], acc[2][0], acc[3][0]);
      *(LAS float*)(L + L_Y + hw * 512 + 256 + lane * 4) = xrow_sum4(acc[4][0], acc[5][0], acc[6][0], acc[7][0]);
    }
    wait_lgkm();
    unsigned old = 0;
    if (lane == 0) old = __hip_atomic_fetch_add((LAS unsigned*)(L + L_CTL + 4 * C_YCNT), 1u, __ATOMIC_RELAXED, __HIP_MEMORY_SCOPE_WORKGROUP);
    old = __builtin_amdgcn_readfirstlane(old);
    asm volatile("" ::: "memory");
    if (old == 3u) {
      if (lane < 32) {
        const f32x4 y0 = *(LAS f32x4*)(L + L_Y + lane * 16), y1 = *(LAS f32x4*)(L + L_Y + 512 + lane * 16);
        const f32x4 y2 = *(LAS f32x4*)(L + L_Y + 1024 + lane * 16), y3 = *(LAS f32x4*)(L + L_Y + 1536 + lane * 16);
        const f32x4 y = (y0 + y1) + (y2 + y3);
        st_sc1(rws, yslot + lane * 16, v4u{tagf(y[0]), tagf(y[1]), tagf(y[2]), tagf(y[3])});
      }
    }
  }
}

}

extern "C" void kernel_launch(void* const* d_in, const int* in_sizes, int n_in, void* d_out, int out_size, void* d_ws, size_t ws_size,
                              hipStream_t stream) {
  Params p;
  p.x = (const unsigned short*)d_in[0]; p.k = (const unsigned short*)d_in[1]; p.v = (const unsigned short*)d_in[2];
  p.wq = (const unsigned short*)d_in[3]; p.wo = (const unsigned short*)d_in[4];
  p.w1 = (const unsigned short*)d_in[6]; p.b1 = (const unsigned short*)d_in[7]; p.w2 = (const unsigned short*)d_in[8]; p.b2 = (const unsigned short*)d_in[9];
  p.out = (unsigned short*)d_out; p.ws = (unsigned char*)d_ws;
  (void)hipFuncSetAttribute((const void*)fused_decode_kernel, hipFuncAttributeMaxDynamicSharedMemorySize, (int)L_END);
  hipLaunchKernelGGL(fused_decode_kernel, dim3(NWG), dim3(NTHR), L_END, stream, p);
}
```
